```python
import torch

def _softmax(x, axis=-1):
    return torch.softmax(x, dim=axis)
def _gelu(x):
    return torch.nn.functional.gelu(x, approximate="tanh")
def _silu(x):
    return torch.nn.functional.silu(x)
def _sigmoid(x):
    return torch.sigmoid(x)
def _layernorm(x, axis=-1, eps=1e-5):
    m = x.mean(dim=axis, keepdim=True)
    v = x.var(dim=axis, keepdim=True, unbiased=False)
    return (x - m) / torch.sqrt(v + eps)
def _scatter_add(tgt, idx, src, axis=0):
    return tgt.clone().index_add_(axis, idx, src)
def _segment_sum(src, seg, n):
    out = torch.zeros((n,) + src.shape[1:], dtype=src.dtype)
    return out.index_add_(0, seg, src)
def _pad(x, pw):
    flat = tuple(v for pair in reversed(pw) for v in pair)
    return torch.nn.functional.pad(x, flat)

kernel_name = 'standalone__self_sup_conv_stem_hubert__speech'

def setup_inputs():

    _w = (lambda a: (lambda t: t.to(torch.bfloat16) if t.is_floating_point() else t)(torch.as_tensor(a)))
    B, T, D, K = 2, 4000, 256, 31
    scale = (1.0 / (K * D)) ** 0.5
    return {
        "x": _w((lambda *s: torch.randn(*s, dtype=torch.bfloat16) * 0.05)(B, T, D)),
        "w": _w((lambda *s: torch.randn(*s, dtype=torch.bfloat16) * 0.05)(K, D, D) * scale),
        "b": _w((lambda *s: torch.randn(*s, dtype=torch.bfloat16) * 0.05)(D) * 0.1),
        "gamma": _w(torch.ones((D,), dtype=float)),
        "beta": _w(torch.zeros((D,), dtype=float)),
    }

def reference(x, w, b, gamma, beta):
    B, T, D = x.shape
    K = w.shape[0]
    s = 2
    L = (T - K) // s + 1
    y = torch.einsum('blc,cd->bld', x[:, 0 : s * L : s, :], w[0])
    for i in range(1, K):
        y = y + torch.einsum('blc,cd->bld', x[:, i : i + s * L : s, :], w[i])
    y = y + b[None, None, :]
    mu = y.mean(1, keepdims=True)
    var = ((y - mu) ** 2).mean(1, keepdims=True)
    yn = (y - mu) / torch.sqrt(var + 1e-5)
    yn = yn * gamma[None, None, :] + beta[None, None, :]
    return _gelu(yn)
```

```cpp
#include <hip/hip_runtime.h>
#include <hip/hip_bf16.h>

constexpr int B_ = 2, T_ = 4000, D_ = 256, KT = 31, L_ = 1985;
constexpr int MT = 128;
constexpr int NT = 32;
constexpr int NMT = 16;
constexpr int PLANE = 144 * 128;
constexpr int XBUF = 2 * PLANE;
constexpr int WSLOTS = 16;
constexpr int WRING = WSLOTS * 1024;
constexpr int LDS_W = 2 * XBUF;
constexpr int LDS_TOTAL = LDS_W + 4 * WRING;
constexpr int NSTEP = 4 * KT;

typedef __attribute__((ext_vector_type(8))) short bf16x8;
typedef __attribute__((ext_vector_type(4))) short s16x4;
typedef __attribute__((ext_vector_type(16))) float f32x16;
typedef __attribute__((ext_vector_type(4))) float f32x4;
typedef __attribute__((address_space(3))) s16x4* lds_s16x4;
typedef __attribute__((address_space(3))) unsigned* lds_u32;

#define WAIT_V(n) asm volatile("s_waitcnt vmcnt(" #n ")" ::: "memory")
#define WAIT_L0() asm volatile("s_waitcnt lgkmcnt(0)" ::: "memory")

__device__ __forceinline__ float bf2f(unsigned short u) { return __uint_as_float(((unsigned)u) << 16); }

__global__ __launch_bounds__(256, 1)
void conv_gemm_kernel(const __hip_bfloat16* __restrict__ xg, const __hip_bfloat16* __restrict__ wg,
                      const __hip_bfloat16* __restrict__ bg, float* __restrict__ y, float* __restrict__ stats) {
  extern __shared__ __attribute__((aligned(16))) char smem[];
  const int tid = threadIdx.x;
  const int lane = tid & 63;
  const int w = __builtin_amdgcn_readfirstlane(tid >> 6);
  const int bid = blockIdx.x;
  const int ntile = bid & 7;
  const int mt = bid >> 3;
  const int b = mt >> 4;
  const int mtile = mt & 15;
  const int l0 = mtile * MT;
  const int n0 = ntile * NT;

  const char* xb = (const char*)(xg + (size_t)b * T_ * D_);
  const char* wb = (const char*)wg;

  auto load_x = [&](int cc, int buf) {
#pragma unroll
    for (int i = 0; i < 9; ++i) {
      const int piece = w * 9 + i;
      const int p = piece >= 18 ? 1 : 0;
      const int u0 = (piece - p * 18) * 8;
      const int u = u0 + (lane >> 3);
      int t = 2 * l0 + 2 * u + p;
      t = t < T_ ? t : T_ - 1;
      const int j = (lane & 7) ^ ((u >> 1) & 7);
      const char* src = xb + (size_t)t * (D_ * 2) + cc * 128 + j * 16;
      char* dst = smem + buf * XBUF + p * PLANE + u0 * 128;
      __builtin_amdgcn_global_load_lds((const unsigned*)src, (lds_u32)dst, 16, 0, 0);
    }
  };
  auto load_w = [&](int step) {
    const int cc = step / KT;
    const int tap = step - cc * KT;
    const int k0 = tap * D_ + cc * 64 + w * 16 + (lane >> 2);
    const char* src = wb + (size_t)k0 * (D_ * 2) + n0 * 2 + (lane & 3) * 16;
    char* dst = smem + LDS_W + w * WRING + (step & (WSLOTS - 1)) * 1024;
    __builtin_amdgcn_global_load_lds((const unsigned*)src, (lds_u32)dst, 16, 0, 0);
  };

  f32x16 acc[4];
#pragma unroll
  for (int m = 0; m < 4; ++m)
#pragma unroll
    for (int i = 0; i < 16; ++i) acc[m][i] = 0.f;

  const int r = lane & 31, h = lane >> 5;
  const int jj = 2 * w + h;
  const int blk = (lane >> 4) & 1, q = (lane & 15) >> 2, pp = lane & 3;
  const int troff = (8 * h + q) * 64 + blk * 32 + pp * 8;

  auto read_frags = [&](int step, bf16x8 (&a)[4], s16x4& b0, s16x4& b1) {
    const int cc = step / KT;
    const int tap = step - cc * KT;
    const int u = r + (tap >> 1);
    const char* ap = smem + (cc & 1) * XBUF + (tap & 1) * PLANE + u * 128 + ((jj ^ ((u >> 1) & 7)) << 4);
#pragma unroll
    for (int m = 0; m < 4; ++m) a[m] = *(const bf16x8*)(ap + m * 4096);
    char* bp = smem + LDS_W + w * WRING + (step & (WSLOTS - 1)) * 1024 + troff;
    b0 = __builtin_amdgcn_ds_read_tr16_b64_v4i16((lds_s16x4)bp);
    b1 = __builtin_amdgcn_ds_read_tr16_b64_v4i16((lds_s16x4)(bp + 256));
  };

  load_x(0, 0);
#pragma unroll
  for (int s = 0; s < WSLOTS - 1; ++s) load_w(s);
  load_x(1, 1);
  WAIT_V(23);
  __builtin_amdgcn_s_barrier();
  bf16x8 a[4]; s16x4 b0, b1;
  read_frags(0, a, b0, b1);

  for (int step = 0; step < NSTEP; ++step) {
    bf16x8 an[4]; s16x4 bn0, bn1;
    const int nx = step + 1;
    if (nx < NSTEP) {
      if (nx % KT == 0) {
        WAIT_V(14);
        WAIT_L0();
        __builtin_amdgcn_s_barrier();
        const int cc = nx / KT;
        if (cc + 1 < 4) load_x(cc + 1, (cc + 1) & 1);
      }
      WAIT_V(14);
      read_frags(nx, an, bn0, bn1);
    }
    if (step + WSLOTS - 1 < NSTEP) load_w(step + WSLOTS - 1);
    else asm volatile("" ::: "memory");
    bf16x8 bf;
    bf[0] = b0[0]; bf[1] = b0[1]; bf[2] = b0[2]; bf[3] = b0[3];
    bf[4] = b1[0]; bf[5] = b1[1]; bf[6] = b1[2]; bf[7] = b1[3];
#pragma unroll
    for (int m = 0; m < 4; ++m) acc[m] = __builtin_amdgcn_mfma_f32_32x32x16_bf16(a[m], bf, acc[m], 0, 0, 0);
#pragma unroll
    for (int m = 0; m < 4; ++m) a[m] = an[m];
    b0 = bn0; b1 = bn1;
  }

  WAIT_V(0);
  WAIT_L0();
  __builtin_amdgcn_s_barrier();
  float* red = (float*)smem;
#pragma unroll
  for (int m = 0; m < 4; ++m)
#pragma unroll
    for (int i = 0; i < 16; ++i) {
      const int row = m * 32 + (i & 3) + 8 * (i >> 2) + 4 * h;
      red[(w * 128 + row) * 32 + r] = acc[m][i];
    }
  __syncthreads();
  const int c4 = (tid & 7) * 4;
  const int rr = tid >> 3;
  f32x4 bias;
#pragma unroll
  for (int e = 0; e < 4; ++e) bias[e] = bf2f(((const unsigned short*)bg)[n0 + c4 + e]);
  f32x4 v[4];
  f32x4 s1 = {0.f, 0.f, 0.f, 0.f};
  const int nvalid = (L_ - l0) < MT ? (L_ - l0) : MT;
#pragma unroll
  for (int i = 0; i < 4; ++i) {
    const int row = rr + 32 * i;
    f32x4 t = bias;
#pragma unroll
    for (int ww = 0; ww < 4; ++ww) t += *(const f32x4*)(red + (ww * 128 + row) * 32 + c4);
    v[i] = t;
    if (row < nvalid) {
      *(f32x4*)(y + ((size_t)(b * L_ + l0 + row)) * D_ + n0 + c4) = t;
      s1 += t;
    }
  }
  __syncthreads();
  float* sred = (float*)(smem);
#pragma unroll
  for (int e = 0; e < 4; ++e) {
    float t = s1[e];
    t += __shfl_xor(t, 8); t += __shfl_xor(t, 16); t += __shfl_xor(t, 32);
    s1[e] = t;
  }
  if (lane < 8) *(f32x4*)(sred + w * 32 + c4) = s1;
  __syncthreads();
  f32x4 mean;
#pragma unroll
  for (int e = 0; e < 4; ++e)
    mean[e] = (sred[c4 + e] + sred[32 + c4 + e] + sred[64 + c4 + e] + sred[96 + c4 + e]) / (float)nvalid;
  f32x4 s2 = {0.f, 0.f, 0.f, 0.f};
#pragma unroll
  for (int i = 0; i < 4; ++i) {
    const int row = rr + 32 * i;
    if (row < nvalid) { f32x4 d = v[i] - mean; s2 += d * d; }
  }
#pragma unroll
  for (int e = 0; e < 4; ++e) {
    float t = s2[e];
    t += __shfl_xor(t, 8); t += __shfl_xor(t, 16); t += __shfl_xor(t, 32);
    s2[e] = t;
  }
  if (lane < 8) *(f32x4*)(sred + 128 + w * 32 + c4) = s2;
  __syncthreads();
  if (tid < 8) {
    f32x4 m2;
#pragma unroll
    for (int e = 0; e < 4; ++e)
      m2[e] = sred[128 + c4 + e] + sred[160 + c4 + e] + sred[192 + c4 + e] + sred[224 + c4 + e];
    float* sp = stats + (size_t)mt * 2 * D_;
    *(f32x4*)(sp + n0 + c4) = mean;
    *(f32x4*)(sp + D_ + n0 + c4) = m2;
  }
}

constexpr int RPB = 16;
constexpr int NB2 = (L_ + RPB - 1) / RPB;

__global__ __launch_bounds__(256)
void norm_gelu_kernel(const float* __restrict__ y, const float* __restrict__ stats,
                      const __hip_bfloat16* __restrict__ gg, const __hip_bfloat16* __restrict__ beg,
                      __hip_bfloat16* __restrict__ out) {
  __shared__ float sc[D_], sh[D_];
  const int tid = threadIdx.x;
  const int b = blockIdx.x / NB2;
  const int rb = blockIdx.x - b * NB2;
  {
    float n = 0.f, mean = 0.f, m2 = 0.f;
#pragma unroll
    for (int t = 0; t < NMT; ++t) {
      const float nb = (float)((L_ - t * MT) < MT ? (L_ - t * MT) : MT);
      const float mb = stats[(size_t)(b * NMT + t) * 2 * D_ + tid];
      const float vb = stats[(size_t)(b * NMT + t) * 2 * D_ + D_ + tid];
      const float nn = n + nb;
      const float d = mb - mean;
      mean += d * (nb / nn);
      m2 += vb + d * d * (n * nb / nn);
      n = nn;
    }
    const float rstd = 1.0f / sqrtf(m2 / (float)L_ + 1e-5f);
    const float g = bf2f(((const unsigned short*)gg)[tid]);
    const float be = bf2f(((const unsigned short*)beg)[tid]);
    sc[tid] = rstd * g;
    sh[tid] = be - mean * rstd * g;
  }
  __syncthreads();
  const int c8 = (tid & 31) * 8;
  float s8[8], h8[8];
#pragma unroll
  for (int e = 0; e < 8; ++e) { s8[e] = sc[c8 + e]; h8[e] = sh[c8 + e]; }
#pragma unroll
  for (int i = 0; i < RPB / 8; ++i) {
    const int l = rb * RPB + i * 8 + (tid >> 5);
    if (l < L_) {
      const size_t off = ((size_t)(b * L_ + l)) * D_ + c8;
      const f32x4 v0 = *(const f32x4*)(y + off);
      const f32x4 v1 = *(const f32x4*)(y + off + 4);
      __hip_bfloat16 o[8];
#pragma unroll
      for (int e = 0; e < 8; ++e) {
        const float yv = e < 4 ? v0[e] : v1[e - 4];
        const float z = yv * s8[e] + h8[e];
        const float inner = 0.7978845608028654f * (z + 0.044715f * z * z * z);
        o[e] = __float2bfloat16(0.5f * z * (1.0f + tanhf(inner)));
      }
      *(uint4*)(out + off) = *(const uint4*)o;
    }
  }
}

extern "C" void kernel_launch(void* const* d_in, const int* in_sizes, int n_in,
                              void* d_out, int out_size, void* d_ws, size_t ws_size,
                              hipStream_t stream) {
  const __hip_bfloat16* x = (const __hip_bfloat16*)d_in[0];
  const __hip_bfloat16* w = (const __hip_bfloat16*)d_in[1];
  const __hip_bfloat16* bb = (const __hip_bfloat16*)d_in[2];
  const __hip_bfloat16* gamma = (const __hip_bfloat16*)d_in[3];
  const __hip_bfloat16* beta = (const __hip_bfloat16*)d_in[4];
  float* y = (float*)d_ws;
  float* stats = y + (size_t)B_ * L_ * D_;
  hipFuncSetAttribute((const void*)conv_gemm_kernel, hipFuncAttributeMaxDynamicSharedMemorySize, LDS_TOTAL);
  conv_gemm_kernel<<<B_ * NMT * 8, 256, LDS_TOTAL, stream>>>(x, w, bb, y, stats);
  norm_gelu_kernel<<<B_ * NB2, 256, 0, stream>>>(y, stats, gamma, beta, (__hip_bfloat16*)d_out);
}
```

```cpp
#include <hip/hip_runtime.h>
#include <hip/hip_bf16.h>

constexpr int B_ = 2, T_ = 4000, D_ = 256, KT = 31, L_ = 1985;
constexpr int MT = 128;
constexpr int NT = 32;
constexpr int NMT = 16;
constexpr int PLANE = 144 * 128;
constexpr int XBUF = 2 * PLANE;
constexpr int WSLOTS = 16;
constexpr int WRING = WSLOTS * 1024;
constexpr int LDS_W = 2 * XBUF;
constexpr int LDS_TOTAL = LDS_W + 4 * WRING;
constexpr int NSTEP = 4 * KT;

typedef __attribute__((ext_vector_type(8))) short bf16x8;
typedef __attribute__((ext_vector_type(4))) short s16x4;
typedef __attribute__((ext_vector_type(16))) float f32x16;
typedef __attribute__((ext_vector_type(4))) float f32x4;
typedef __attribute__((address_space(3))) s16x4* lds_s16x4;
typedef __attribute__((address_space(3))) unsigned* lds_u32;

#define WAIT_V(n) asm volatile("s_waitcnt vmcnt(" #n ")" ::: "memory")
#define WAIT_L0() asm volatile("s_waitcnt lgkmcnt(0)" ::: "memory")

__device__ __forceinline__ void glds16(const void* src, unsigned ldsaddr) {
  unsigned keep;
  asm volatile("s_mov_b32 %0, m0\n\ts_mov_b32 m0, %2\n\ts_nop 0\n\tglobal_load_lds_dwordx4 %1, off\n\ts_mov_b32 m0, %0"
               : "=&s"(keep) : "v"(src), "s"(ldsaddr) : "memory");
}
__device__ __forceinline__ float bf2f(unsigned short u) { return __uint_as_float(((unsigned)u) << 16); }

__global__ __launch_bounds__(256, 1)
void conv_gemm_kernel(const __hip_bfloat16* __restrict__ xg, const __hip_bfloat16* __restrict__ wg,
                      const __hip_bfloat16* __restrict__ bg, float* __restrict__ y, float* __restrict__ stats) {
  extern __shared__ __attribute__((aligned(16))) char smem[];
  const int tid = threadIdx.x;
  const int lane = tid & 63;
  const int w = __builtin_amdgcn_readfirstlane(tid >> 6);
  const int bid = blockIdx.x;
  const int ntile = bid & 7;
  const int mt = bid >> 3;
  const int b = mt >> 4;
  const int mtile = mt & 15;
  const int l0 = mtile * MT;
  const int n0 = ntile * NT;

  const char* xb = (const char*)(xg + (size_t)b * T_ * D_);
  const char* wb = (const char*)wg;
  const unsigned lbase = (unsigned)(size_t)smem;

  auto load_x = [&](int cc, int buf) {
#pragma unroll
    for (int i = 0; i < 9; ++i) {
      const int piece = w * 9 + i;
      const int p = piece >= 18 ? 1 : 0;
      const int u0 = (piece - p * 18) * 8;
      const int u = u0 + (lane >> 3);
      int t = 2 * l0 + 2 * u + p;
      t = t < T_ ? t : T_ - 1;
      const int j = (lane & 7) ^ ((u >> 1) & 7);
      const char* src = xb + (size_t)t * (D_ * 2) + cc * 128 + j * 16;
      glds16(src, __builtin_amdgcn_readfirstlane(lbase + buf * XBUF + p * PLANE + u0 * 128));
    }
  };
  auto load_w = [&](int step) {
    const int cc = step / KT;
    const int tap = step - cc * KT;
    const int k0 = tap * D_ + cc * 64 + w * 16 + (lane >> 2);
    const char* src = wb + (size_t)k0 * (D_ * 2) + n0 * 2 + (lane & 3) * 16;
    glds16(src, __builtin_amdgcn_readfirstlane(lbase + LDS_W + w * WRING + (step & (WSLOTS - 1)) * 1024));
  };

  f32x16 acc[4];
#pragma unroll
  for (int m = 0; m < 4; ++m)
#pragma unroll
    for (int i = 0; i < 16; ++i) acc[m][i] = 0.f;

  const int r = lane & 31, h = lane >> 5;
  const int jj = 2 * w + h;
  const int blk = (lane >> 4) & 1, q = (lane & 15) >> 2, pp = lane & 3;
  const int troff = (8 * h + q) * 64 + blk * 32 + pp * 8;

  auto read_frags = [&](int step, bf16x8 (&a)[4], s16x4& b0, s16x4& b1) {
    const int cc = step / KT;
    const int tap = step - cc * KT;
    const int u = r + (tap >> 1);
    const char* ap = smem + (cc & 1) * XBUF + (tap & 1) * PLANE + u * 128 + ((jj ^ ((u >> 1) & 7)) << 4);
#pragma unroll
    for (int m = 0; m < 4; ++m) a[m] = *(const bf16x8*)(ap + m * 4096);
    char* bp = smem + LDS_W + w * WRING + (step & (WSLOTS - 1)) * 1024 + troff;
    b0 = __builtin_amdgcn_ds_read_tr16_b64_v4i16((lds_s16x4)bp);
    b1 = __builtin_amdgcn_ds_read_tr16_b64_v4i16((lds_s16x4)(bp + 256));
  };

  load_x(0, 0);
#pragma unroll
  for (int s = 0; s < WSLOTS - 1; ++s) load_w(s);
  load_x(1, 1);
  WAIT_V(23);
  __builtin_amdgcn_s_barrier();
  bf16x8 a[4]; s16x4 b0, b1;
  read_frags(0, a, b0, b1);

  for (int step = 0; step < NSTEP; ++step) {
    bf16x8 an[4]; s16x4 bn0, bn1;
    const int nx = step + 1;
    if (nx < NSTEP) {
      if (nx % KT == 0) {
        WAIT_V(14);
        WAIT_L0();
        __builtin_amdgcn_s_barrier();
        const int cc = nx / KT;
        if (cc + 1 < 4) load_x(cc + 1, (cc + 1) & 1);
      }
      if (nx + WSLOTS - 2 >= NSTEP) WAIT_V(0); else WAIT_V(14);
      read_frags(nx, an, bn0, bn1);
    }
    if (step + WSLOTS - 1 < NSTEP) load_w(step + WSLOTS - 1);
    else asm volatile("" ::: "memory");
    bf16x8 bf;
    bf[0] = b0[0]; bf[1] = b0[1]; bf[2] = b0[2]; bf[3] = b0[3];
    bf[4] = b1[0]; bf[5] = b1[1]; bf[6] = b1[2]; bf[7] = b1[3];
#pragma unroll
    for (int m = 0; m < 4; ++m) acc[m] = __builtin_amdgcn_mfma_f32_32x32x16_bf16(a[m], bf, acc[m], 0, 0, 0);
#pragma unroll
    for (int m = 0; m < 4; ++m) a[m] = an[m];
    b0 = bn0; b1 = bn1;
  }

  WAIT_V(0);
  WAIT_L0();
  __builtin_amdgcn_s_barrier();
  float* red = (float*)smem;
#pragma unroll
  for (int m = 0; m < 4; ++m)
#pragma unroll
    for (int i = 0; i < 16; ++i) {
      const int row = m * 32 + (i & 3) + 8 * (i >> 2) + 4 * h;
      red[(w * 128 + row) * 32 + r] = acc[m][i];
    }
  __syncthreads();
  const int c4 = (tid & 7) * 4;
  const int rr = tid >> 3;
  f32x4 bias;
#pragma unroll
  for (int e = 0; e < 4; ++e) bias[e] = bf2f(((const unsigned short*)bg)[n0 + c4 + e]);
  f32x4 v[4];
  f32x4 s1 = {0.f, 0.f, 0.f, 0.f};
  const int nvalid = (L_ - l0) < MT ? (L_ - l0) : MT;
#pragma unroll
  for (int i = 0; i < 4; ++i) {
    const int row = rr + 32 * i;
    f32x4 t = bias;
#pragma unroll
    for (int ww = 0; ww < 4; ++ww) t += *(const f32x4*)(red + (ww * 128 + row) * 32 + c4);
    v[i] = t;
    if (row < nvalid) {
      *(f32x4*)(y + ((size_t)(b * L_ + l0 + row)) * D_ + n0 + c4) = t;
      s1 += t;
    }
  }
  __syncthreads();
  float* sred = (float*)(smem);
#pragma unroll
  for (int e = 0; e < 4; ++e) {
    float t = s1[e];
    t += __shfl_xor(t, 8); t += __shfl_xor(t, 16); t += __shfl_xor(t, 32);
    s1[e] = t;
  }
  if (lane < 8) *(f32x4*)(sred + w * 32 + c4) = s1;
  __syncthreads();
  f32x4 mean;
#pragma unroll
  for (int e = 0; e < 4; ++e)
    mean[e] = (sred[c4 + e] + sred[32 + c4 + e] + sred[64 + c4 + e] + sred[96 + c4 + e]) / (float)nvalid;
  f32x4 s2 = {0.f, 0.f, 0.f, 0.f};
#pragma unroll
  for (int i = 0; i < 4; ++i) {
    const int row = rr + 32 * i;
    if (row < nvalid) { f32x4 d = v[i] - mean; s2 += d * d; }
  }
#pragma unroll
  for (int e = 0; e < 4; ++e) {
    float t = s2[e];
    t += __shfl_xor(t, 8); t += __shfl_xor(t, 16); t += __shfl_xor(t, 32);
    s2[e] = t;
  }
  if (lane < 8) *(f32x4*)(sred + 128 + w * 32 + c4) = s2;
  __syncthreads();
  if (tid < 8) {
    f32x4 m2;
#pragma unroll
    for (int e = 0; e < 4; ++e)
      m2[e] = sred[128 + c4 + e] + sred[160 + c4 + e] + sred[192 + c4 + e] + sred[224 + c4 + e];
    float* sp = stats + (size_t)mt * 2 * D_;
    *(f32x4*)(sp + n0 + c4) = mean;
    *(f32x4*)(sp + D_ + n0 + c4) = m2;
  }
}

constexpr int RPB = 16;
constexpr int NB2 = (L_ + RPB - 1) / RPB;

__global__ __launch_bounds__(256)
void norm_gelu_kernel(const float* __restrict__ y, const float* __restrict__ stats,
                      const __hip_bfloat16* __restrict__ gg, const __hip_bfloat16* __restrict__ beg,
                      __hip_bfloat16* __restrict__ out) {
  __shared__ float sc[D_], sh[D_];
  const int tid = threadIdx.x;
  const int b = blockIdx.x / NB2;
  const int rb = blockIdx.x - b * NB2;
  {
    float n = 0.f, mean = 0.f, m2 = 0.f;
#pragma unroll
    for (int t = 0; t < NMT; ++t) {
      const float nb = (float)((L_ - t * MT) < MT ? (L_ - t * MT) : MT);
      const float mb = stats[(size_t)(b * NMT + t) * 2 * D_ + tid];
      const float vb = stats[(size_t)(b * NMT + t) * 2 * D_ + D_ + tid];
      const float nn = n + nb;
      const float d = mb - mean;
      mean += d * (nb / nn);
      m2 += vb + d * d * (n * nb / nn);
      n = nn;
    }
    const float rstd = 1.0f / sqrtf(m2 / (float)L_ + 1e-5f);
    const float g = bf2f(((const unsigned short*)gg)[tid]);
    const float be = bf2f(((const unsigned short*)beg)[tid]);
    sc[tid] = rstd * g;
    sh[tid] = be - mean * rstd * g;
  }
  __syncthreads();
  const int c8 = (tid & 31) * 8;
  float s8[8], h8[8];
#pragma unroll
  for (int e = 0; e < 8; ++e) { s8[e] = sc[c8 + e]; h8[e] = sh[c8 + e]; }
#pragma unroll
  for (int i = 0; i < RPB / 8; ++i) {
    const int l = rb * RPB + i * 8 + (tid >> 5);
    if (l < L_) {
      const size_t off = ((size_t)(b * L_ + l)) * D_ + c8;
      const f32x4 v0 = *(const f32x4*)(y + off);
      const f32x4 v1 = *(const f32x4*)(y + off + 4);
      __hip_bfloat16 o[8];
#pragma unroll
      for (int e = 0; e < 8; ++e) {
        const float yv = e < 4 ? v0[e] : v1[e - 4];
        const float z = yv * s8[e] + h8[e];
        const float inner = 0.7978845608028654f * (z + 0.044715f * z * z * z);
        o[e] = __float2bfloat16(0.5f * z * (1.0f + tanhf(inner)));
      }
      *(uint4*)(out + off) = *(const uint4*)o;
    }
  }
}

extern "C" void kernel_launch(void* const* d_in, const int* in_sizes, int n_in,
                              void* d_out, int out_size, void* d_ws, size_t ws_size,
                              hipStream_t stream) {
  const __hip_bfloat16* x = (const __hip_bfloat16*)d_in[0];
  const __hip_bfloat16* w = (const __hip_bfloat16*)d_in[1];
  const __hip_bfloat16* bb = (const __hip_bfloat16*)d_in[2];
  const __hip_bfloat16* gamma = (const __hip_bfloat16*)d_in[3];
  const __hip_bfloat16* beta = (const __hip_bfloat16*)d_in[4];
  float* y = (float*)d_ws;
  float* stats = y + (size_t)B_ * L_ * D_;
  hipFuncSetAttribute((const void*)conv_gemm_kernel, hipFuncAttributeMaxDynamicSharedMemorySize, LDS_TOTAL);
  conv_gemm_kernel<<<B_ * NMT * 8, 256, LDS_TOTAL, stream>>>(x, w, bb, y, stats);
  norm_gelu_kernel<<<B_ * NB2, 256, 0, stream>>>(y, stats, gamma, beta, (__hip_bfloat16*)d_out);
}
```

```cpp
#include <hip/hip_runtime.h>
#include <hip/hip_bf16.h>

constexpr int B_ = 2, T_ = 4000, D_ = 256, KT = 31, L_ = 1985;
constexpr int MT = 128;
constexpr int NT = 32;
constexpr int NMT = 16;
constexpr int PLANE = 144 * 128;
constexpr int XBUF = 2 * PLANE;
constexpr int WSLOTS = 16;
constexpr int WRING = WSLOTS * 1024;
constexpr int LDS_W = 2 * XBUF;
constexpr int LDS_TOTAL = LDS_W + 4 * WRING;
constexpr int NSTEP = 4 * KT;

typedef __attribute__((ext_vector_type(8))) short bf16x8;
typedef __attribute__((ext_vector_type(4))) short s16x4;
typedef __attribute__((ext_vector_type(16))) float f32x16;
typedef __attribute__((ext_vector_type(4))) float f32x4;
typedef __attribute__((address_space(3))) s16x4* lds_s16x4;
typedef __attribute__((address_space(3))) unsigned* lds_u32;

#define WAIT_V(n) asm volatile("s_waitcnt vmcnt(" #n ")" ::: "memory")
#define WAIT_L0() asm volatile("s_waitcnt lgkmcnt(0)" ::: "memory")

__device__ __forceinline__ void glds16(const void* src, unsigned ldsaddr) {
  unsigned keep;
  asm volatile("s_mov_b32 %0, m0\n\ts_mov_b32 m0, %2\n\ts_nop 0\n\tglobal_load_lds_dwordx4 %1, off\n\ts_mov_b32 m0, %0"
               : "=&s"(keep) : "v"(src), "s"(ldsaddr) : "memory");
}
template <int I> struct IC { static constexpr int value = I; };
template <int I, int N, class F> __device__ __forceinline__ void static_for(F&& f) {
  if constexpr (I < N) { f(IC<I>{}); static_for<I + 1, N>(f); }
}
__device__ __forceinline__ float bf2f(unsigned short u) { return __uint_as_float(((unsigned)u) << 16); }

__global__ __launch_bounds__(256, 1)
void conv_gemm_kernel(const __hip_bfloat16* __restrict__ xg, const __hip_bfloat16* __restrict__ wg,
                      const __hip_bfloat16* __restrict__ bg, float* __restrict__ y, float* __restrict__ stats) {
  extern __shared__ __attribute__((aligned(16))) char smem[];
  const int tid = threadIdx.x;
  const int lane = tid & 63;
  const int w = __builtin_amdgcn_readfirstlane(tid >> 6);
  const int bid = blockIdx.x;
  const int ntile = bid & 7;
  const int mt = bid >> 3;
  const int b = mt >> 4;
  const int mtile = mt & 15;
  const int l0 = mtile * MT;
  const int n0 = ntile * NT;

  const char* xb = (const char*)(xg + (size_t)b * T_ * D_);
  const char* wb = (const char*)wg;
  const unsigned lbase = (unsigned)(size_t)smem;

  auto load_x = [&](int cc, int buf) {
#pragma unroll
    for (int i = 0; i < 9; ++i) {
      const int piece = w * 9 + i;
      const int p = piece >= 18 ? 1 : 0;
      const int u0 = (piece - p * 18) * 8;
      const int u = u0 + (lane >> 3);
      int t = 2 * l0 + 2 * u + p;
      t = t < T_ ? t : T_ - 1;
      const int j = (lane & 7) ^ ((u >> 1) & 7);
      const char* src = xb + (size_t)t * (D_ * 2) + cc * 128 + j * 16;
      glds16(src, __builtin_amdgcn_readfirstlane(lbase + buf * XBUF + p * PLANE + u0 * 128));
    }
  };
  const unsigned wvoff = (unsigned)((w * 16 + (lane >> 2)) * (D_ * 2) + n0 * 2 + (lane & 3) * 16);
  const unsigned wring = __builtin_amdgcn_readfirstlane(lbase + LDS_W + w * WRING);
  auto load_w = [&](int step) {
    const int cc = step / KT;
    const int tap = step - cc * KT;
    const char* sb = wb + (size_t)(tap * D_ + cc * 64) * (D_ * 2);
    asm volatile("s_mov_b32 m0, %2\n\ts_nop 0\n\tglobal_load_lds_dwordx4 %0, %1"
                 :: "v"(wvoff), "s"(sb), "s"(wring + (step & (WSLOTS - 1)) * 1024) : "memory");
  };

  f32x16 acc[4];
#pragma unroll
  for (int m = 0; m < 4; ++m)
#pragma unroll
    for (int i = 0; i < 16; ++i) acc[m][i] = 0.f;

  const int r = lane & 31, h = lane >> 5;
  const int jj = 2 * w + h;
  const int blk = (lane >> 4) & 1, q = (lane & 15) >> 2, pp = lane & 3;
  char* const bbase = smem + LDS_W + w * WRING + (8 * h + q) * 64 + blk * 32 + pp * 8;
  int aoff[2][16];
#pragma unroll
  for (int th = 0; th < 16; ++th) {
    const int u = r + th;
    aoff[0][th] = u * 128 + ((jj ^ ((u >> 1) & 7)) << 4);
    aoff[1][th] = aoff[0][th] + XBUF;
  }

  auto read_frags = [&](int step, bf16x8 (&a)[4], s16x4& b0, s16x4& b1) {
    const int cc = step / KT;
    const int tap = step - cc * KT;
    const char* ap = smem + aoff[cc & 1][tap >> 1] + (tap & 1) * PLANE;
#pragma unroll
    for (int m = 0; m < 4; ++m) a[m] = *(const bf16x8*)(ap + m * 4096);
    char* bp = bbase + (step & (WSLOTS - 1)) * 1024;
    b0 = __builtin_amdgcn_ds_read_tr16_b64_v4i16((lds_s16x4)bp);
    b1 = __builtin_amdgcn_ds_read_tr16_b64_v4i16((lds_s16x4)(bp + 256));
  };

  load_x(0, 0);
#pragma unroll
  for (int s = 0; s < WSLOTS - 1; ++s) load_w(s);
  load_x(1, 1);
  WAIT_V(23);
  __builtin_amdgcn_s_barrier();
  bf16x8 a[2][4]; s16x4 b0[2], b1[2];
  read_frags(0, a[0], b0[0], b1[0]);

  static_for<0, NSTEP>([&](auto STEP) {
    constexpr int step = decltype(STEP)::value;
    constexpr int nx = step + 1;
    constexpr int cur = step & 1;
    constexpr bool pf = nx < NSTEP;
    constexpr int ncc = nx / KT, ntap = nx - ncc * KT;
    if constexpr (pf && ntap == 0) {
      WAIT_V(14);
      WAIT_L0();
      __builtin_amdgcn_s_barrier();
      if (ncc + 1 < 4) load_x(ncc + 1, (ncc + 1) & 1);
    }
    const char* ap = smem + aoff[ncc & 1][ntap >> 1] + (ntap & 1) * PLANE;
    char* bp = bbase + (nx & (WSLOTS - 1)) * 1024;
    bf16x8 bf;
    bf[0] = b0[cur][0]; bf[1] = b0[cur][1]; bf[2] = b0[cur][2]; bf[3] = b0[cur][3];
    bf[4] = b1[cur][0]; bf[5] = b1[cur][1]; bf[6] = b1[cur][2]; bf[7] = b1[cur][3];
#define SB __builtin_amdgcn_sched_barrier(0)
    SB;
    acc[0] = __builtin_amdgcn_mfma_f32_32x32x16_bf16(a[cur][0], bf, acc[0], 0, 0, 0);
    SB;
    if constexpr (pf) { a[cur ^ 1][0] = *(const bf16x8*)(ap); a[cur ^ 1][1] = *(const bf16x8*)(ap + 4096); }
    SB;
    acc[1] = __builtin_amdgcn_mfma_f32_32x32x16_bf16(a[cur][1], bf, acc[1], 0, 0, 0);
    SB;
    if constexpr (pf) { a[cur ^ 1][2] = *(const bf16x8*)(ap + 8192); a[cur ^ 1][3] = *(const bf16x8*)(ap + 12288); }
    SB;
    acc[2] = __builtin_amdgcn_mfma_f32_32x32x16_bf16(a[cur][2], bf, acc[2], 0, 0, 0);
    SB;
    if constexpr (pf) {
      if (nx + WSLOTS - 2 >= NSTEP) WAIT_V(0); else WAIT_V(14);
      b0[cur ^ 1] = __builtin_amdgcn_ds_read_tr16_b64_v4i16((lds_s16x4)bp);
      b1[cur ^ 1] = __builtin_amdgcn_ds_read_tr16_b64_v4i16((lds_s16x4)(bp + 256));
    }
    SB;
    acc[3] = __builtin_amdgcn_mfma_f32_32x32x16_bf16(a[cur][3], bf, acc[3], 0, 0, 0);
    SB;
    if (step + WSLOTS - 1 < NSTEP) load_w(step + WSLOTS - 1);
    SB;
  });

  WAIT_V(0);
  WAIT_L0();
  __builtin_amdgcn_s_barrier();
  float* red = (float*)smem;
#pragma unroll
  for (int m = 0; m < 4; ++m)
#pragma unroll
    for (int i = 0; i < 16; ++i) {
      const int row = m * 32 + (i & 3) + 8 * (i >> 2) + 4 * h;
      red[(w * 128 + row) * 32 + r] = acc[m][i];
    }
  __syncthreads();
  const int c4 = (tid & 7) * 4;
  const int rr = tid >> 3;
  f32x4 bias;
#pragma unroll
  for (int e = 0; e < 4; ++e) bias[e] = bf2f(((const unsigned short*)bg)[n0 + c4 + e]);
  f32x4 v[4];
  f32x4 s1 = {0.f, 0.f, 0.f, 0.f};
  const int nvalid = (L_ - l0) < MT ? (L_ - l0) : MT;
#pragma unroll
  for (int i = 0; i < 4; ++i) {
    const int row = rr + 32 * i;
    f32x4 t = bias;
#pragma unroll
    for (int ww = 0; ww < 4; ++ww) t += *(const f32x4*)(red + (ww * 128 + row) * 32 + c4);
    v[i] = t;
    if (row < nvalid) {
      *(f32x4*)(y + ((size_t)(b * L_ + l0 + row)) * D_ + n0 + c4) = t;
      s1 += t;
    }
  }
  __syncthreads();
  float* sred = (float*)(smem);
#pragma unroll
  for (int e = 0; e < 4; ++e) {
    float t = s1[e];
    t += __shfl_xor(t, 8); t += __shfl_xor(t, 16); t += __shfl_xor(t, 32);
    s1[e] = t;
  }
  if (lane < 8) *(f32x4*)(sred + w * 32 + c4) = s1;
  __syncthreads();
  f32x4 mean;
#pragma unroll
  for (int e = 0; e < 4; ++e)
    mean[e] = (sred[c4 + e] + sred[32 + c4 + e] + sred[64 + c4 + e] + sred[96 + c4 + e]) / (float)nvalid;
  f32x4 s2 = {0.f, 0.f, 0.f, 0.f};
#pragma unroll
  for (int i = 0; i < 4; ++i) {
    const int row = rr + 32 * i;
    if (row < nvalid) { f32x4 d = v[i] - mean; s2 += d * d; }
  }
#pragma unroll
  for (int e = 0; e < 4; ++e) {
    float t = s2[e];
    t += __shfl_xor(t, 8); t += __shfl_xor(t, 16); t += __shfl_xor(t, 32);
    s2[e] = t;
  }
  if (lane < 8) *(f32x4*)(sred + 128 + w * 32 + c4) = s2;
  __syncthreads();
  if (tid < 8) {
    f32x4 m2;
#pragma unroll
    for (int e = 0; e < 4; ++e)
      m2[e] = sred[128 + c4 + e] + sred[160 + c4 + e] + sred[192 + c4 + e] + sred[224 + c4 + e];
    float* sp = stats + (size_t)mt * 2 * D_;
    *(f32x4*)(sp + n0 + c4) = mean;
    *(f32x4*)(sp + D_ + n0 + c4) = m2;
  }
}

constexpr int RPB = 8;
constexpr int NB2 = (L_ + RPB - 1) / RPB;

__global__ __launch_bounds__(256)
void norm_gelu_kernel(const float* __restrict__ y, const float* __restrict__ stats,
                      const __hip_bfloat16* __restrict__ gg, const __hip_bfloat16* __restrict__ beg,
                      __hip_bfloat16* __restrict__ out) {
  __shared__ float sc[D_], sh[D_];
  const int tid = threadIdx.x;
  const int b = blockIdx.x / NB2;
  const int rb = blockIdx.x - b * NB2;
  {
    float n = 0.f, mean = 0.f, m2 = 0.f;
#pragma unroll
    for (int t = 0; t < NMT; ++t) {
      const float nb = (float)((L_ - t * MT) < MT ? (L_ - t * MT) : MT);
      const float mb = stats[(size_t)(b * NMT + t) * 2 * D_ + tid];
      const float vb = stats[(size_t)(b * NMT + t) * 2 * D_ + D_ + tid];
      const float nn = n + nb;
      const float d = mb - mean;
      mean += d * (nb / nn);
      m2 += vb + d * d * (n * nb / nn);
      n = nn;
    }
    const float rstd = 1.0f / sqrtf(m2 / (float)L_ + 1e-5f);
    const float g = bf2f(((const unsigned short*)gg)[tid]);
    const float be = bf2f(((const unsigned short*)beg)[tid]);
    sc[tid] = rstd * g;
    sh[tid] = be - mean * rstd * g;
  }
  __syncthreads();
  const int c8 = (tid & 31) * 8;
  float s8[8], h8[8];
#pragma unroll
  for (int e = 0; e < 8; ++e) { s8[e] = sc[c8 + e]; h8[e] = sh[c8 + e]; }
#pragma unroll
  for (int i = 0; i < RPB / 8; ++i) {
    const int l = rb * RPB + i * 8 + (tid >> 5);
    if (l < L_) {
      const size_t off = ((size_t)(b * L_ + l)) * D_ + c8;
      const f32x4 v0 = *(const f32x4*)(y + off);
      const f32x4 v1 = *(const f32x4*)(y + off + 4);
      __hip_bfloat16 o[8];
#pragma unroll
      for (int e = 0; e < 8; ++e) {
        const float yv = e < 4 ? v0[e] : v1[e - 4];
        const float z = yv * s8[e] + h8[e];
        const float inner = 0.7978845608028654f * (z + 0.044715f * z * z * z);
        o[e] = __float2bfloat16(z / (1.0f + __expf(-2.0f * inner)));
      }
      *(uint4*)(out + off) = *(const uint4*)o;
    }
  }
}

extern "C" void kernel_launch(void* const* d_in, const int* in_sizes, int n_in,
                              void* d_out, int out_size, void* d_ws, size_t ws_size,
                              hipStream_t stream) {
  const __hip_bfloat16* x = (const __hip_bfloat16*)d_in[0];
  const __hip_bfloat16* w = (const __hip_bfloat16*)d_in[1];
  const __hip_bfloat16* bb = (const __hip_bfloat16*)d_in[2];
  const __hip_bfloat16* gamma = (const __hip_bfloat16*)d_in[3];
  const __hip_bfloat16* beta = (const __hip_bfloat16*)d_in[4];
  float* y = (float*)d_ws;
  float* stats = y + (size_t)B_ * L_ * D_;
  hipFuncSetAttribute((const void*)conv_gemm_kernel, hipFuncAttributeMaxDynamicSharedMemorySize, LDS_TOTAL);
  conv_gemm_kernel<<<B_ * NMT * 8, 256, LDS_TOTAL, stream>>>(x, w, bb, y, stats);
  norm_gelu_kernel<<<B_ * NB2, 256, 0, stream>>>(y, stats, gamma, beta, (__hip_bfloat16*)d_out);
}
```

```cpp
#include <hip/hip_runtime.h>
#include <hip/hip_bf16.h>

constexpr int B_ = 2, T_ = 4000, D_ = 256, KT = 31, L_ = 1985;
constexpr int MT = 128;
constexpr int NT = 32;
constexpr int NMT = 16;
constexpr int PLANE = 144 * 128;
constexpr int XBUF = 2 * PLANE;
constexpr int WSLOTS = 16;
constexpr int WRING = WSLOTS * 1024;
constexpr int LDS_W = 2 * XBUF;
constexpr int LDS_TOTAL = LDS_W + 4 * WRING;
constexpr int NSTEP = 4 * KT;

typedef __attribute__((ext_vector_type(8))) short bf16x8;
typedef __attribute__((ext_vector_type(4))) short s16x4;
typedef __attribute__((ext_vector_type(16))) float f32x16;
typedef __attribute__((ext_vector_type(4))) float f32x4;
typedef __attribute__((address_space(3))) s16x4* lds_s16x4;
typedef __attribute__((address_space(3))) unsigned* lds_u32;

#define WAIT_V(n) asm volatile("s_waitcnt vmcnt(" #n ")" ::: "memory")
#define WAIT_L0() asm volatile("s_waitcnt lgkmcnt(0)" ::: "memory")

__device__ __forceinline__ void glds16(const void* src, unsigned ldsaddr) {
  unsigned keep;
  asm volatile("s_mov_b32 %0, m0\n\ts_mov_b32 m0, %2\n\ts_nop 0\n\tglobal_load_lds_dwordx4 %1, off\n\ts_mov_b32 m0, %0"
               : "=&s"(keep) : "v"(src), "s"(ldsaddr) : "memory");
}
template <int I> struct IC { static constexpr int value = I; };
template <int I, int N, class F> __device__ __forceinline__ void static_for(F&& f) {
  if constexpr (I < N) { f(IC<I>{}); static_for<I + 1, N>(f); }
}
__device__ __forceinline__ float bf2f(unsigned short u) { return __uint_as_float(((unsigned)u) << 16); }

__global__ __launch_bounds__(256, 1)
void conv_gemm_kernel(const __hip_bfloat16* __restrict__ xg, const __hip_bfloat16* __restrict__ wg,
                      const __hip_bfloat16* __restrict__ bg, float* __restrict__ y, float* __restrict__ stats) {
  extern __shared__ __attribute__((aligned(16))) char smem[];
  const int tid = threadIdx.x;
  const int lane = tid & 63;
  const int w = __builtin_amdgcn_readfirstlane(tid >> 6);
  const int bid = blockIdx.x;
  const int ntile = bid & 7;
  const int mt = bid >> 3;
  const int b = mt >> 4;
  const int mtile = mt & 15;
  const int l0 = mtile * MT;
  const int n0 = ntile * NT;

  const char* xb = (const char*)(xg + (size_t)b * T_ * D_);
  const char* wb = (const char*)wg;
  const unsigned lbase = (unsigned)(size_t)smem;

  auto load_x = [&](int cc, int buf) {
#pragma unroll
    for (int i = 0; i < 9; ++i) {
      const int piece = w * 9 + i;
      const int p = piece >= 18 ? 1 : 0;
      const int u0 = (piece - p * 18) * 8;
      const int u = u0 + (lane >> 3);
      int t = 2 * l0 + 2 * u + p;
      t = t < T_ ? t : T_ - 1;
      const int j = (lane & 7) ^ ((u >> 1) & 7);
      const char* src = xb + (size_t)t * (D_ * 2) + cc * 128 + j * 16;
      glds16(src, __builtin_amdgcn_readfirstlane(lbase + buf * XBUF + p * PLANE + u0 * 128));
    }
  };
  const unsigned wvoff = (unsigned)((w * 16 + (lane >> 2)) * (D_ * 2) + n0 * 2 + (lane & 3) * 16);
  const unsigned wring = __builtin_amdgcn_readfirstlane(lbase + LDS_W + w * WRING);
  auto load_w = [&](int cc, int tap) {
    const char* sb = wb + (size_t)(tap * D_ + cc * 64) * (D_ * 2);
    asm volatile("s_mov_b32 m0, %2\n\ts_nop 0\n\tglobal_load_lds_dwordx4 %0, %1"
                 :: "v"(wvoff), "s"(sb), "s"(wring + (tap & (WSLOTS - 1)) * 1024) : "memory");
  };

  f32x16 acc[4];
#pragma unroll
  for (int m = 0; m < 4; ++m)
#pragma unroll
    for (int i = 0; i < 16; ++i) acc[m][i] = 0.f;

  const int r = lane & 31, h = lane >> 5;
  const int jj = 2 * w + h;
  const int blk = (lane >> 4) & 1, q = (lane & 15) >> 2, pp = lane & 3;
  char* const bbase = smem + LDS_W + w * WRING + (8 * h + q) * 64 + blk * 32 + pp * 8;
  int aoff[16];
#pragma unroll
  for (int th = 0; th < 16; ++th) {
    const int u = r + th;
    aoff[th] = u * 128 + ((jj ^ ((u >> 1) & 7)) << 4);
  }

  load_x(0, 0);
#pragma unroll
  for (int s = 0; s < WSLOTS - 1; ++s) load_w(0, s);
  load_x(1, 1);
  WAIT_V(23);
  __builtin_amdgcn_s_barrier();
  bf16x8 a[2][4]; s16x4 b0[2], b1[2];
#pragma unroll
  for (int m = 0; m < 4; ++m) a[0][m] = *(const bf16x8*)(smem + aoff[0] + m * 4096);
  b0[0] = __builtin_amdgcn_ds_read_tr16_b64_v4i16((lds_s16x4)bbase);
  b1[0] = __builtin_amdgcn_ds_read_tr16_b64_v4i16((lds_s16x4)(bbase + 256));

#define SB __builtin_amdgcn_sched_barrier(0)
#pragma unroll 1
  for (int cc = 0; cc < 4; ++cc) {
    const int ccn = (cc + 1) & 3;
    static_for<0, KT>([&](auto TAP) {
      constexpr int tap = decltype(TAP)::value;
      constexpr int cur = tap & 1;
      constexpr int ntap = (tap + 1) % KT;
      constexpr int nb = (tap + 1 == KT) ? 0 : (cur ^ 1);
      if constexpr (ntap == 0) {
        WAIT_V(14);
        WAIT_L0();
        __builtin_amdgcn_s_barrier();
        load_x((cc + 2) & 3, cc & 1);
#pragma unroll
        for (int th = 0; th < 16; ++th) aoff[th] += (cc & 1) ? -XBUF : XBUF;
      }
      const char* ap = smem + aoff[ntap >> 1] + (ntap & 1) * PLANE;
      char* bp = bbase + (ntap & (WSLOTS - 1)) * 1024;
      bf16x8 bf;
      bf[0] = b0[cur][0]; bf[1] = b0[cur][1]; bf[2] = b0[cur][2]; bf[3] = b0[cur][3];
      bf[4] = b1[cur][0]; bf[5] = b1[cur][1]; bf[6] = b1[cur][2]; bf[7] = b1[cur][3];
      bf16x8 c0 = a[cur][0], c1 = a[cur][1], c2 = a[cur][2], c3 = a[cur][3];
      SB;
      acc[0] = __builtin_amdgcn_mfma_f32_32x32x16_bf16(c0, bf, acc[0], 0, 0, 0);
      SB;
      a[nb][0] = *(const bf16x8*)(ap);
      SB;
      acc[1] = __builtin_amdgcn_mfma_f32_32x32x16_bf16(c1, bf, acc[1], 0, 0, 0);
      SB;
      a[nb][1] = *(const bf16x8*)(ap + 4096);
      SB;
      acc[2] = __builtin_amdgcn_mfma_f32_32x32x16_bf16(c2, bf, acc[2], 0, 0, 0);
      SB;
      a[nb][2] = *(const bf16x8*)(ap + 8192);
      SB;
      acc[3] = __builtin_amdgcn_mfma_f32_32x32x16_bf16(c3, bf, acc[3], 0, 0, 0);
      SB;
      a[nb][3] = *(const bf16x8*)(ap + 12288);
      WAIT_V(13);
      b0[nb] = __builtin_amdgcn_ds_read_tr16_b64_v4i16((lds_s16x4)bp);
      b1[nb] = __builtin_amdgcn_ds_read_tr16_b64_v4i16((lds_s16x4)(bp + 256));
      if constexpr (tap + WSLOTS - 1 < KT) load_w(cc, tap + WSLOTS - 1);
      else load_w(ccn, tap + WSLOTS - 1 - KT);
      SB;
    });
  }

  WAIT_V(0);
  WAIT_L0();
  __builtin_amdgcn_s_barrier();
  float* red = (float*)smem;
#pragma unroll
  for (int m = 0; m < 4; ++m)
#pragma unroll
    for (int i = 0; i < 16; ++i) {
      const int row = m * 32 + (i & 3) + 8 * (i >> 2) + 4 * h;
      red[(w * 128 + row) * 32 + r] = acc[m][i];
    }
  __syncthreads();
  const int c4 = (tid & 7) * 4;
  const int rr = tid >> 3;
  f32x4 bias;
#pragma unroll
  for (int e = 0; e < 4; ++e) bias[e] = bf2f(((const unsigned short*)bg)[n0 + c4 + e]);
  f32x4 v[4];
  f32x4 s1 = {0.f, 0.f, 0.f, 0.f};
  const int nvalid = (L_ - l0) < MT ? (L_ - l0) : MT;
#pragma unroll
  for (int i = 0; i < 4; ++i) {
    const int row = rr + 32 * i;
    f32x4 t = bias;
#pragma unroll
    for (int ww = 0; ww < 4; ++ww) t += *(const f32x4*)(red + (ww * 128 + row) * 32 + c4);
    v[i] = t;
    if (row < nvalid) {
      *(f32x4*)(y + ((size_t)(b * L_ + l0 + row)) * D_ + n0 + c4) = t;
      s1 += t;
    }
  }
  __syncthreads();
  float* sred = (float*)(smem);
#pragma unroll
  for (int e = 0; e < 4; ++e) {
    float t = s1[e];
    t += __shfl_xor(t, 8); t += __shfl_xor(t, 16); t += __shfl_xor(t, 32);
    s1[e] = t;
  }
  if (lane < 8) *(f32x4*)(sred + w * 32 + c4) = s1;
  __syncthreads();
  f32x4 mean;
#pragma unroll
  for (int e = 0; e < 4; ++e)
    mean[e] = (sred[c4 + e] + sred[32 + c4 + e] + sred[64 + c4 + e] + sred[96 + c4 + e]) / (float)nvalid;
  f32x4 s2 = {0.f, 0.f, 0.f, 0.f};
#pragma unroll
  for (int i = 0; i < 4; ++i) {
    const int row = rr + 32 * i;
    if (row < nvalid) { f32x4 d = v[i] - mean; s2 += d * d; }
  }
#pragma unroll
  for (int e = 0; e < 4; ++e) {
    float t = s2[e];
    t += __shfl_xor(t, 8); t += __shfl_xor(t, 16); t += __shfl_xor(t, 32);
    s2[e] = t;
  }
  if (lane < 8) *(f32x4*)(sred + 128 + w * 32 + c4) = s2;
  __syncthreads();
  if (tid < 8) {
    f32x4 m2;
#pragma unroll
    for (int e = 0; e < 4; ++e)
      m2[e] = sred[128 + c4 + e] + sred[160 + c4 + e] + sred[192 + c4 + e] + sred[224 + c4 + e];
    float* sp = stats + (size_t)mt * 2 * D_;
    *(f32x4*)(sp + n0 + c4) = mean;
    *(f32x4*)(sp + D_ + n0 + c4) = m2;
  }
}

constexpr int RPB = 8;
constexpr int NB2 = (L_ + RPB - 1) / RPB;

__global__ __launch_bounds__(256)
void norm_gelu_kernel(const float* __restrict__ y, const float* __restrict__ stats,
                      const __hip_bfloat16* __restrict__ gg, const __hip_bfloat16* __restrict__ beg,
                      __hip_bfloat16* __restrict__ out) {
  __shared__ float sc[D_], sh[D_];
  const int tid = threadIdx.x;
  const int b = blockIdx.x / NB2;
  const int rb = blockIdx.x - b * NB2;
  {
    float n = 0.f, mean = 0.f, m2 = 0.f;
#pragma unroll
    for (int t = 0; t < NMT; ++t) {
      const float nb = (float)((L_ - t * MT) < MT ? (L_ - t * MT) : MT);
      const float mb = stats[(size_t)(b * NMT + t) * 2 * D_ + tid];
      const float vb = stats[(size_t)(b * NMT + t) * 2 * D_ + D_ + tid];
      const float nn = n + nb;
      const float d = mb - mean;
      mean += d * (nb / nn);
      m2 += vb + d * d * (n * nb / nn);
      n = nn;
    }
    const float rstd = 1.0f / sqrtf(m2 / (float)L_ + 1e-5f);
    const float g = bf2f(((const unsigned short*)gg)[tid]);
    const float be = bf2f(((const unsigned short*)beg)[tid]);
    sc[tid] = rstd * g;
    sh[tid] = be - mean * rstd * g;
  }
  __syncthreads();
  const int c8 = (tid & 31) * 8;
  float s8[8], h8[8];
#pragma unroll
  for (int e = 0; e < 8; ++e) { s8[e] = sc[c8 + e]; h8[e] = sh[c8 + e]; }
#pragma unroll
  for (int i = 0; i < RPB / 8; ++i) {
    const int l = rb * RPB + i * 8 + (tid >> 5);
    if (l < L_) {
      const size_t off = ((size_t)(b * L_ + l)) * D_ + c8;
      const f32x4 v0 = *(const f32x4*)(y + off);
      const f32x4 v1 = *(const f32x4*)(y + off + 4);
      __hip_bfloat16 o[8];
#pragma unroll
      for (int e = 0; e < 8; ++e) {
        const float yv = e < 4 ? v0[e] : v1[e - 4];
        const float z = yv * s8[e] + h8[e];
        const float inner = 0.7978845608028654f * (z + 0.044715f * z * z * z);
        o[e] = __float2bfloat16(z / (1.0f + __expf(-2.0f * inner)));
      }
      *(uint4*)(out + off) = *(const uint4*)o;
    }
  }
}

extern "C" void kernel_launch(void* const* d_in, const int* in_sizes, int n_in,
                              void* d_out, int out_size, void* d_ws, size_t ws_size,
                              hipStream_t stream) {
  const __hip_bfloat16* x = (const __hip_bfloat16*)d_in[0];
  const __hip_bfloat16* w = (const __hip_bfloat16*)d_in[1];
  const __hip_bfloat16* bb = (const __hip_bfloat16*)d_in[2];
  const __hip_bfloat16* gamma = (const __hip_bfloat16*)d_in[3];
  const __hip_bfloat16* beta = (const __hip_bfloat16*)d_in[4];
  float* y = (float*)d_ws;
  float* stats = y + (size_t)B_ * L_ * D_;
  hipFuncSetAttribute((const void*)conv_gemm_kernel, hipFuncAttributeMaxDynamicSharedMemorySize, LDS_TOTAL);
  conv_gemm_kernel<<<B_ * NMT * 8, 256, LDS_TOTAL, stream>>>(x, w, bb, y, stats);
  norm_gelu_kernel<<<B_ * NB2, 256, 0, stream>>>(y, stats, gamma, beta, (__hip_bfloat16*)d_out);
}
```

```cpp
#include <hip/hip_runtime.h>
#include <hip/hip_bf16.h>

constexpr int B_ = 2, T_ = 4000, D_ = 256, KT = 31, L_ = 1985;
constexpr int MT = 128;
constexpr int NT = 32;
constexpr int NMT = 16;
constexpr int PLANE = 144 * 128;
constexpr int XBUF = 2 * PLANE;
constexpr int WSLOTS = 16;
constexpr int WRING = WSLOTS * 1024;
constexpr int LDS_W = 2 * XBUF;
constexpr int LDS_TOTAL = LDS_W + 4 * WRING;
constexpr int NSTEP = 4 * KT;

typedef __attribute__((ext_vector_type(8))) short bf16x8;
typedef __attribute__((ext_vector_type(4))) short s16x4;
typedef __attribute__((ext_vector_type(16))) float f32x16;
typedef __attribute__((ext_vector_type(4))) float f32x4;
typedef __attribute__((address_space(3))) s16x4* lds_s16x4;
typedef __attribute__((address_space(3))) unsigned* lds_u32;

#define WAIT_V(n) asm volatile("s_waitcnt vmcnt(" #n ")" ::: "memory")
#define WAIT_L0() asm volatile("s_waitcnt lgkmcnt(0)" ::: "memory")

__device__ __forceinline__ void glds16(const void* src, unsigned ldsaddr) {
  unsigned keep;
  asm volatile("s_mov_b32 %0, m0\n\ts_mov_b32 m0, %2\n\ts_nop 0\n\tglobal_load_lds_dwordx4 %1, off\n\ts_mov_b32 m0, %0"
               : "=&s"(keep) : "v"(src), "s"(ldsaddr) : "memory");
}
template <int I> struct IC { static constexpr int value = I; };
template <int I, int N, class F> __device__ __forceinline__ void static_for(F&& f) {
  if constexpr (I < N) { f(IC<I>{}); static_for<I + 1, N>(f); }
}
__device__ __forceinline__ float bf2f(unsigned short u) { return __uint_as_float(((unsigned)u) << 16); }

__global__ __launch_bounds__(256, 1)
void conv_gemm_kernel(const __hip_bfloat16* __restrict__ xg, const __hip_bfloat16* __restrict__ wg,
                      const __hip_bfloat16* __restrict__ bg, float* __restrict__ y, float* __restrict__ stats) {
  extern __shared__ __attribute__((aligned(16))) char smem[];
  const int tid = threadIdx.x;
  const int lane = tid & 63;
  const int w = __builtin_amdgcn_readfirstlane(tid >> 6);
  const int bid = blockIdx.x;
  const int ntile = bid & 7;
  const int mt = bid >> 3;
  const int b = mt >> 4;
  const int mtile = mt & 15;
  const int l0 = mtile * MT;
  const int n0 = ntile * NT;

  const char* xb = (const char*)(xg + (size_t)b * T_ * D_);
  const char* wb = (const char*)wg;
  const unsigned lbase = (unsigned)(size_t)smem;

  auto load_x = [&](int cc, int buf) {
#pragma unroll
    for (int i = 0; i < 9; ++i) {
      const int piece = w * 9 + i;
      const int p = piece >= 18 ? 1 : 0;
      const int u0 = (piece - p * 18) * 8;
      const int u = u0 + (lane >> 3);
      int t = 2 * l0 + 2 * u + p;
      t = t < T_ ? t : T_ - 1;
      const int j = (lane & 7) ^ ((u >> 1) & 7);
      const char* src = xb + (size_t)t * (D_ * 2) + cc * 128 + j * 16;
      glds16(src, __builtin_amdgcn_readfirstlane(lbase + buf * XBUF + p * PLANE + u0 * 128));
    }
  };
  const unsigned wvoff = (unsigned)((w * 16 + (lane >> 2)) * (D_ * 2) + n0 * 2 + (lane & 3) * 16);
  const unsigned wring = __builtin_amdgcn_readfirstlane(lbase + LDS_W + w * WRING);
  auto load_w = [&](int cc, int tap) {
    const char* sb = wb + (size_t)(tap * D_ + cc * 64) * (D_ * 2);
    asm volatile("s_mov_b32 m0, %2\n\ts_nop 0\n\tglobal_load_lds_dwordx4 %0, %1"
                 :: "v"(wvoff), "s"(sb), "s"(wring + (tap & (WSLOTS - 1)) * 1024) : "memory");
  };

  f32x16 acc[4];
#pragma unroll
  for (int m = 0; m < 4; ++m)
#pragma unroll
    for (int i = 0; i < 16; ++i) acc[m][i] = 0.f;

  const int r = lane & 31, h = lane >> 5;
  const int jj = 2 * w + h;
  const int blk = (lane >> 4) & 1, q = (lane & 15) >> 2, pp = lane & 3;
  char* const bbase = smem + LDS_W + w * WRING + (8 * h + q) * 64 + blk * 32 + pp * 8;
  int aoff[16];
#pragma unroll
  for (int th = 0; th < 16; ++th) {
    const int u = r + th;
    aoff[th] = u * 128 + ((jj ^ ((u >> 1) & 7)) << 4);
  }

  load_x(0, 0);

#pragma unroll
  for (int s = 0; s < WSLOTS - 1; ++s) load_w(0, s);
  load_x(1, 1);
  {
    unsigned sink = 0;
#pragma unroll
    for (int i = 0; i < 4; ++i) {
      int kr = mt * 248 + w * 62 + i * 16 + (lane >> 2);
      kr = kr < KT * D_ ? kr : KT * D_ - 1;
      sink ^= *(const unsigned*)(wb + (size_t)kr * (D_ * 2) + n0 * 2 + (lane & 3) * 16);
    }
    asm volatile("" :: "v"(sink));
  }
  WAIT_V(23);
  __builtin_amdgcn_s_barrier();
  bf16x8 a[2][4]; s16x4 b0[2], b1[2];
#pragma unroll
  for (int m = 0; m < 4; ++m) a[0][m] = *(const bf16x8*)(smem + aoff[0] + m * 4096);
  b0[0] = __builtin_amdgcn_ds_read_tr16_b64_v4i16((lds_s16x4)bbase);
  b1[0] = __builtin_amdgcn_ds_read_tr16_b64_v4i16((lds_s16x4)(bbase + 256));

#define SB __builtin_amdgcn_sched_barrier(0)
#pragma unroll 1
  for (int cc = 0; cc < 4; ++cc) {
    const int ccn = (cc + 1) & 3;
    static_for<0, KT>([&](auto TAP) {
      constexpr int tap = decltype(TAP)::value;
      constexpr int cur = tap & 1;
      constexpr int ntap = (tap + 1) % KT;
      constexpr int nb = (tap + 1 == KT) ? 0 : (cur ^ 1);
      if constexpr (ntap == 0) {
        WAIT_V(14);
        WAIT_L0();
        __builtin_amdgcn_s_barrier();
        load_x((cc + 2) & 3, cc & 1);
#pragma unroll
        for (int th = 0; th < 16; ++th) aoff[th] += (cc & 1) ? -XBUF : XBUF;
      }
      const char* ap = smem + aoff[ntap >> 1] + (ntap & 1) * PLANE;
      char* bp = bbase + (ntap & (WSLOTS - 1)) * 1024;
      bf16x8 bf;
      bf[0] = b0[cur][0]; bf[1] = b0[cur][1]; bf[2] = b0[cur][2]; bf[3] = b0[cur][3];
      bf[4] = b1[cur][0]; bf[5] = b1[cur][1]; bf[6] = b1[cur][2]; bf[7] = b1[cur][3];
      bf16x8 c0 = a[cur][0], c1 = a[cur][1], c2 = a[cur][2], c3 = a[cur][3];
      SB;
      acc[0] = __builtin_amdgcn_mfma_f32_32x32x16_bf16(c0, bf, acc[0], 0, 0, 0);
      SB;
      WAIT_V(13);
      b0[nb] = __builtin_amdgcn_ds_read_tr16_b64_v4i16((lds_s16x4)bp);
      b1[nb] = __builtin_amdgcn_ds_read_tr16_b64_v4i16((lds_s16x4)(bp + 256));
      a[nb][0] = *(const bf16x8*)(ap);
      SB;
      acc[1] = __builtin_amdgcn_mfma_f32_32x32x16_bf16(c1, bf, acc[1], 0, 0, 0);
      SB;
      a[nb][1] = *(const bf16x8*)(ap + 4096);
      a[nb][2] = *(const bf16x8*)(ap + 8192);
      SB;
      acc[2] = __builtin_amdgcn_mfma_f32_32x32x16_bf16(c2, bf, acc[2], 0, 0, 0);
      SB;
      a[nb][3] = *(const bf16x8*)(ap + 12288);
      SB;
      acc[3] = __builtin_amdgcn_mfma_f32_32x32x16_bf16(c3, bf, acc[3], 0, 0, 0);
      SB;
      if constexpr (tap + WSLOTS - 1 < KT) load_w(cc, tap + WSLOTS - 1);
      else load_w(ccn, tap + WSLOTS - 1 - KT);
      asm volatile("" :: "v"(c0), "v"(c1), "v"(c2), "v"(c3), "v"(bf));
      SB;
    });
  }

  WAIT_V(0);
  WAIT_L0();
  __builtin_amdgcn_s_barrier();
  float* red = (float*)smem;
#pragma unroll
  for (int m = 0; m < 4; ++m)
#pragma unroll
    for (int i = 0; i < 16; ++i) {
      const int row = m * 32 + (i & 3) + 8 * (i >> 2) + 4 * h;
      red[(w * 128 + row) * 32 + r] = acc[m][i];
    }
  __syncthreads();
  const int c4 = (tid & 7) * 4;
  const int rr = tid >> 3;
  f32x4 bias;
#pragma unroll
  for (int e = 0; e < 4; ++e) bias[e] = bf2f(((const unsigned short*)bg)[n0 + c4 + e]);
  f32x4 v[4];
  f32x4 s1 = {0.f, 0.f, 0.f, 0.f};
  const int nvalid = (L_ - l0) < MT ? (L_ - l0) : MT;
#pragma unroll
  for (int i = 0; i < 4; ++i) {
    const int row = rr + 32 * i;
    f32x4 t = bias;
#pragma unroll
    for (int ww = 0; ww < 4; ++ww) t += *(const f32x4*)(red + (ww * 128 + row) * 32 + c4);
    v[i] = t;
    if (row < nvalid) {
      *(f32x4*)(y + ((size_t)(b * L_ + l0 + row)) * D_ + n0 + c4) = t;
      s1 += t;
    }
  }
  __syncthreads();
  float* sred = (float*)(smem);
#pragma unroll
  for (int e = 0; e < 4; ++e) {
    float t = s1[e];
    t += __shfl_xor(t, 8); t += __shfl_xor(t, 16); t += __shfl_xor(t, 32);
    s1[e] = t;
  }
  if (lane < 8) *(f32x4*)(sred + w * 32 + c4) = s1;
  __syncthreads();
  f32x4 mean;
#pragma unroll
  for (int e = 0; e < 4; ++e)
    mean[e] = (sred[c4 + e] + sred[32 + c4 + e] + sred[64 + c4 + e] + sred[96 + c4 + e]) / (float)nvalid;
  f32x4 s2 = {0.f, 0.f, 0.f, 0.f};
#pragma unroll
  for (int i = 0; i < 4; ++i) {
    const int row = rr + 32 * i;
    if (row < nvalid) { f32x4 d = v[i] - mean; s2 += d * d; }
  }
#pragma unroll
  for (int e = 0; e < 4; ++e) {
    float t = s2[e];
    t += __shfl_xor(t, 8); t += __shfl_xor(t, 16); t += __shfl_xor(t, 32);
    s2[e] = t;
  }
  if (lane < 8) *(f32x4*)(sred + 128 + w * 32 + c4) = s2;
  __syncthreads();
  if (tid < 8) {
    f32x4 m2;
#pragma unroll
    for (int e = 0; e < 4; ++e)
      m2[e] = sred[128 + c4 + e] + sred[160 + c4 + e] + sred[192 + c4 + e] + sred[224 + c4 + e];
    float* sp = stats + (size_t)mt * 2 * D_;
    *(f32x4*)(sp + n0 + c4) = mean;
    *(f32x4*)(sp + D_ + n0 + c4) = m2;
  }
}

constexpr int RPB = 8;
constexpr int NB2 = (L_ + RPB - 1) / RPB;

__global__ __launch_bounds__(256)
void norm_gelu_kernel(const float* __restrict__ y, const float* __restrict__ stats,
                      const __hip_bfloat16* __restrict__ gg, const __hip_bfloat16* __restrict__ beg,
                      __hip_bfloat16* __restrict__ out) {
  __shared__ float sc[D_], sh[D_];
  const int tid = threadIdx.x;
  const int b = blockIdx.x / NB2;
  const int rb = blockIdx.x - b * NB2;
  {
    float n = 0.f, mean = 0.f, m2 = 0.f;
#pragma unroll
    for (int t = 0; t < NMT; ++t) {
      const float nb = (float)((L_ - t * MT) < MT ? (L_ - t * MT) : MT);
      const float mb = stats[(size_t)(b * NMT + t) * 2 * D_ + tid];
      const float vb = stats[(size_t)(b * NMT + t) * 2 * D_ + D_ + tid];
      const float nn = n + nb;
      const float d = mb - mean;
      mean += d * (nb / nn);
      m2 += vb + d * d * (n * nb / nn);
      n = nn;
    }
    const float rstd = 1.0f / sqrtf(m2 / (float)L_ + 1e-5f);
    const float g = bf2f(((const unsigned short*)gg)[tid]);
    const float be = bf2f(((const unsigned short*)beg)[tid]);
    sc[tid] = rstd * g;
    sh[tid] = be - mean * rstd * g;
  }
  __syncthreads();
  const int c8 = (tid & 31) * 8;
  float s8[8], h8[8];
#pragma unroll
  for (int e = 0; e < 8; ++e) { s8[e] = sc[c8 + e]; h8[e] = sh[c8 + e]; }
#pragma unroll
  for (int i = 0; i < RPB / 8; ++i) {
    const int l = rb * RPB + i * 8 + (tid >> 5);
    if (l < L_) {
      const size_t off = ((size_t)(b * L_ + l)) * D_ + c8;
      const f32x4 v0 = *(const f32x4*)(y + off);
      const f32x4 v1 = *(const f32x4*)(y + off + 4);
      __hip_bfloat16 o[8];
#pragma unroll
      for (int e = 0; e < 8; ++e) {
        const float yv = e < 4 ? v0[e] : v1[e - 4];
        const float z = yv * s8[e] + h8[e];
        const float inner = 0.7978845608028654f * (z + 0.044715f * z * z * z);
        o[e] = __float2bfloat16(z / (1.0f + __expf(-2.0f * inner)));
      }
      *(uint4*)(out + off) = *(const uint4*)o;
    }
  }
}

extern "C" void kernel_launch(void* const* d_in, const int* in_sizes, int n_in,
                              void* d_out, int out_size, void* d_ws, size_t ws_size,
                              hipStream_t stream) {
  const __hip_bfloat16* x = (const __hip_bfloat16*)d_in[0];
  const __hip_bfloat16* w = (const __hip_bfloat16*)d_in[1];
  const __hip_bfloat16* bb = (const __hip_bfloat16*)d_in[2];
  const __hip_bfloat16* gamma = (const __hip_bfloat16*)d_in[3];
  const __hip_bfloat16* beta = (const __hip_bfloat16*)d_in[4];
  float* y = (float*)d_ws;
  float* stats = y + (size_t)B_ * L_ * D_;
  hipFuncSetAttribute((const void*)conv_gemm_kernel, hipFuncAttributeMaxDynamicSharedMemorySize, LDS_TOTAL);
  conv_gemm_kernel<<<B_ * NMT * 8, 256, LDS_TOTAL, stream>>>(x, w, bb, y, stats);
  norm_gelu_kernel<<<B_ * NB2, 256, 0, stream>>>(y, stats, gamma, beta, (__hip_bfloat16*)d_out);
}
```

```cpp
#include <hip/hip_runtime.h>
#include <hip/hip_bf16.h>

constexpr int B_ = 2, T_ = 4000, D_ = 256, KT = 31, L_ = 1985;
constexpr int MT = 128;
constexpr int NT = 32;
constexpr int NMT = 16;
constexpr int PLANE = 144 * 128;
constexpr int XBUF = 2 * PLANE;
constexpr int WSLOTS = 16;
constexpr int WRING = WSLOTS * 1024;
constexpr int LDS_W = 2 * XBUF;
constexpr int LDS_SCR = LDS_W + 4 * WRING;
constexpr int LDS_TOTAL = LDS_SCR + 4096;
constexpr int NSTEP = 4 * KT;

typedef __attribute__((ext_vector_type(8))) short bf16x8;
typedef __attribute__((ext_vector_type(4))) short s16x4;
typedef __attribute__((ext_vector_type(16))) float f32x16;
typedef __attribute__((ext_vector_type(4))) float f32x4;
typedef __attribute__((address_space(3))) s16x4* lds_s16x4;
typedef __attribute__((address_space(3))) unsigned* lds_u32;

#define WAIT_V(n) asm volatile("s_waitcnt vmcnt(" #n ")" ::: "memory")
#define WAIT_L0() asm volatile("s_waitcnt lgkmcnt(0)" ::: "memory")

__device__ __forceinline__ void glds16(const void* src, unsigned ldsaddr) {
  unsigned keep;
  asm volatile("s_mov_b32 %0, m0\n\ts_mov_b32 m0, %2\n\ts_nop 0\n\tglobal_load_lds_dwordx4 %1, off\n\ts_mov_b32 m0, %0"
               : "=&s"(keep) : "v"(src), "s"(ldsaddr) : "memory");
}
template <int I> struct IC { static constexpr int value = I; };
template <int I, int N, class F> __device__ __forceinline__ void static_for(F&& f) {
  if constexpr (I < N) { f(IC<I>{}); static_for<I + 1, N>(f); }
}
#define STAMP(i)
__device__ __forceinline__ float bf2f(unsigned short u) { return __uint_as_float(((unsigned)u) << 16); }

__global__ __launch_bounds__(256, 1)
void conv_gemm_kernel(const __hip_bfloat16* __restrict__ xg, const __hip_bfloat16* __restrict__ wg,
                      const __hip_bfloat16* __restrict__ bg, float* __restrict__ y, float* __restrict__ stats) {
  extern __shared__ __attribute__((aligned(16))) char smem[];
  const int tid = threadIdx.x;
  const int lane = tid & 63;
  const int w = __builtin_amdgcn_readfirstlane(tid >> 6);
  const int bid = blockIdx.x;
  const int ntile = bid & 7;
  const int mt = bid >> 3;
  const int b = mt >> 4;
  const int mtile = mt & 15;
  const int l0 = mtile * MT;
  const int n0 = ntile * NT;
  STAMP(0);

  const char* xb = (const char*)(xg + (size_t)b * T_ * D_);
  const char* wb = (const char*)wg;
  const unsigned lbase = (unsigned)(size_t)smem;

  auto load_x = [&](int cc, int buf) {
#pragma unroll
    for (int i = 0; i < 9; ++i) {
      const int piece = w * 9 + i;
      const int p = piece >= 18 ? 1 : 0;
      const int u0 = (piece - p * 18) * 8;
      const int u = u0 + (lane >> 3);
      int t = 2 * l0 + 2 * u + p;
      t = t < T_ ? t : T_ - 1;
      const int j = (lane & 7) ^ ((u >> 1) & 7);
      const char* src = xb + (size_t)t * (D_ * 2) + cc * 128 + j * 16;
      glds16(src, __builtin_amdgcn_readfirstlane(lbase + buf * XBUF + p * PLANE + u0 * 128));
    }
  };
  const unsigned wvoff = (unsigned)((w * 16 + (lane >> 2)) * (D_ * 2) + n0 * 2 + (lane & 3) * 16);
  const unsigned wring = __builtin_amdgcn_readfirstlane(lbase + LDS_W + w * WRING);
  auto load_w = [&](int cc, int tap) {
    const char* sb = wb + (size_t)(tap * D_ + cc * 64) * (D_ * 2);
    asm volatile("s_mov_b32 m0, %2\n\ts_nop 0\n\tglobal_load_lds_dwordx4 %0, %1"
                 :: "v"(wvoff), "s"(sb), "s"(wring + (tap & (WSLOTS - 1)) * 1024) : "memory");
  };

  f32x16 acc[4];
#pragma unroll
  for (int m = 0; m < 4; ++m)
#pragma unroll
    for (int i = 0; i < 16; ++i) acc[m][i] = 0.f;

  const int r = lane & 31, h = lane >> 5;
  const int jj = 2 * w + h;
  const int blk = (lane >> 4) & 1, q = (lane & 15) >> 2, pp = lane & 3;
  char* const bbase = smem + LDS_W + w * WRING + (8 * h + q) * 64 + blk * 32 + pp * 8;
  int aoff[16];
#pragma unroll
  for (int th = 0; th < 16; ++th) {
    const int u = r + th;
    aoff[th] = u * 128 + ((jj ^ ((u >> 1) & 7)) << 4);
  }

  load_x(0, 0);

#pragma unroll
  for (int s = 0; s < WSLOTS - 1; ++s) load_w(0, s);
  load_x(1, 1);
#pragma unroll
  for (int i = 0; i < 4; ++i) {
    int kr = mt * 248 + w * 62 + i * 16 + (lane >> 2);
    kr = kr < KT * D_ ? kr : KT * D_ - 1;
    glds16(wb + (size_t)kr * (D_ * 2) + n0 * 2 + (lane & 3) * 16,
           __builtin_amdgcn_readfirstlane(lbase + LDS_SCR + w * 1024));
  }
  WAIT_V(27);
  __builtin_amdgcn_s_barrier();
  STAMP(1);
  bf16x8 a[2][4]; s16x4 b0[2], b1[2];
#pragma unroll
  for (int m = 0; m < 4; ++m) a[0][m] = *(const bf16x8*)(smem + aoff[0] + m * 4096);
  b0[0] = __builtin_amdgcn_ds_read_tr16_b64_v4i16((lds_s16x4)bbase);
  b1[0] = __builtin_amdgcn_ds_read_tr16_b64_v4i16((lds_s16x4)(bbase + 256));

#define SB __builtin_amdgcn_sched_barrier(0)
#pragma unroll 1
  for (int cc = 0; cc < 4; ++cc) {
    const int ccn = (cc + 1) & 3;
    static_for<0, KT>([&](auto TAP) {
      constexpr int tap = decltype(TAP)::value;
      constexpr int cur = tap & 1;
      constexpr int ntap = (tap + 1) % KT;
      constexpr int nb = (tap + 1 == KT) ? 0 : (cur ^ 1);
      if constexpr (ntap == 0) {
        WAIT_V(14);
        WAIT_L0();
        __builtin_amdgcn_s_barrier();
        if (cc < 2) load_x(cc + 2, cc & 1);
#pragma unroll
        for (int th = 0; th < 16; ++th) aoff[th] += (cc & 1) ? -XBUF : XBUF;
      }
      const char* ap = smem + aoff[ntap >> 1] + (ntap & 1) * PLANE;
      char* bp = bbase + (ntap & (WSLOTS - 1)) * 1024;
      bf16x8 bf;
      bf[0] = b0[cur][0]; bf[1] = b0[cur][1]; bf[2] = b0[cur][2]; bf[3] = b0[cur][3];
      bf[4] = b1[cur][0]; bf[5] = b1[cur][1]; bf[6] = b1[cur][2]; bf[7] = b1[cur][3];
      bf16x8 c0 = a[cur][0], c1 = a[cur][1], c2 = a[cur][2], c3 = a[cur][3];
      SB;
      acc[0] = __builtin_amdgcn_mfma_f32_32x32x16_bf16(c0, bf, acc[0], 0, 0, 0);
      SB;
      WAIT_V(13);
      b0[nb] = __builtin_amdgcn_ds_read_tr16_b64_v4i16((lds_s16x4)bp);
      b1[nb] = __builtin_amdgcn_ds_read_tr16_b64_v4i16((lds_s16x4)(bp + 256));
      a[nb][0] = *(const bf16x8*)(ap);
      SB;
      acc[1] = __builtin_amdgcn_mfma_f32_32x32x16_bf16(c1, bf, acc[1], 0, 0, 0);
      SB;
      a[nb][1] = *(const bf16x8*)(ap + 4096);
      a[nb][2] = *(const bf16x8*)(ap + 8192);
      SB;
      acc[2] = __builtin_amdgcn_mfma_f32_32x32x16_bf16(c2, bf, acc[2], 0, 0, 0);
      SB;
      a[nb][3] = *(const bf16x8*)(ap + 12288);
      SB;
      acc[3] = __builtin_amdgcn_mfma_f32_32x32x16_bf16(c3, bf, acc[3], 0, 0, 0);
      SB;
      if constexpr (tap + WSLOTS - 1 < KT) load_w(cc, tap + WSLOTS - 1);
      else load_w(ccn, tap + WSLOTS - 1 - KT);
      asm volatile("" :: "v"(c0), "v"(c1), "v"(c2), "v"(c3), "v"(bf));
      SB;
    });
  }

  STAMP(2);
#define LBAR() do { WAIT_L0(); __builtin_amdgcn_s_barrier(); } while (0)
  LBAR();
  float* red = (float*)smem;
#pragma unroll
  for (int m = 0; m < 4; ++m)
#pragma unroll
    for (int i = 0; i < 16; ++i) {
      const int row = m * 32 + (i & 3) + 8 * (i >> 2) + 4 * h;
      red[(w * 128 + row) * 32 + r] = acc[m][i];
    }
  const int c4 = (tid & 7) * 4;
  const int rr = tid >> 3;
  f32x4 bias;
  {
    const uint2 bu = *(const uint2*)((const unsigned short*)bg + n0 + c4);
    bias[0] = __uint_as_float(bu.x << 16); bias[1] = __uint_as_float(bu.x & 0xffff0000u);
    bias[2] = __uint_as_float(bu.y << 16); bias[3] = __uint_as_float(bu.y & 0xffff0000u);
  }
  LBAR();
  f32x4 v[4];
  f32x4 s1 = {0.f, 0.f, 0.f, 0.f};
  const int nvalid = (L_ - l0) < MT ? (L_ - l0) : MT;
#pragma unroll
  for (int i = 0; i < 4; ++i) {
    const int row = rr + 32 * i;
    f32x4 t = bias;
#pragma unroll
    for (int ww = 0; ww < 4; ++ww) t += *(const f32x4*)(red + (ww * 128 + row) * 32 + c4);
    v[i] = t;
    if (row < nvalid) s1 += t;
  }
  float* sred = (float*)(smem + 65536);
#pragma unroll
  for (int e = 0; e < 4; ++e) {
    float t = s1[e];
    t += __shfl_xor(t, 8); t += __shfl_xor(t, 16); t += __shfl_xor(t, 32);
    s1[e] = t;
  }
  if (lane < 8) *(f32x4*)(sred + w * 32 + c4) = s1;
  LBAR();
  f32x4 mean;
#pragma unroll
  for (int e = 0; e < 4; ++e)
    mean[e] = (sred[c4 + e] + sred[32 + c4 + e] + sred[64 + c4 + e] + sred[96 + c4 + e]) / (float)nvalid;
  f32x4 s2 = {0.f, 0.f, 0.f, 0.f};
#pragma unroll
  for (int i = 0; i < 4; ++i) {
    const int row = rr + 32 * i;
    if (row < nvalid) {
      f32x4 d = v[i] - mean; s2 += d * d;
      *(f32x4*)(y + ((size_t)(b * L_ + l0 + row)) * D_ + n0 + c4) = v[i];
    }
  }
#pragma unroll
  for (int e = 0; e < 4; ++e) {
    float t = s2[e];
    t += __shfl_xor(t, 8); t += __shfl_xor(t, 16); t += __shfl_xor(t, 32);
    s2[e] = t;
  }
  if (lane < 8) *(f32x4*)(sred + 128 + w * 32 + c4) = s2;
  LBAR();
  if (tid < 8) {
    f32x4 m2;
#pragma unroll
    for (int e = 0; e < 4; ++e)
      m2[e] = sred[128 + c4 + e] + sred[160 + c4 + e] + sred[192 + c4 + e] + sred[224 + c4 + e];
    float* sp = stats + (size_t)mt * 2 * D_;
    *(f32x4*)(sp + n0 + c4) = mean;
    *(f32x4*)(sp + D_ + n0 + c4) = m2;
  }
  WAIT_V(0);
  STAMP(3);
}

constexpr int RPB = 8;
constexpr int NB2 = (L_ + RPB - 1) / RPB;

__global__ __launch_bounds__(256)
void norm_gelu_kernel(const float* __restrict__ y, const float* __restrict__ stats,
                      const __hip_bfloat16* __restrict__ gg, const __hip_bfloat16* __restrict__ beg,
                      __hip_bfloat16* __restrict__ out) {
  __shared__ float sc[D_], sh[D_];
  const int tid = threadIdx.x;
  const int b = blockIdx.x / NB2;
  const int rb = blockIdx.x - b * NB2;
  {
    float n = 0.f, mean = 0.f, m2 = 0.f;
#pragma unroll
    for (int t = 0; t < NMT; ++t) {
      const float nb = (float)((L_ - t * MT) < MT ? (L_ - t * MT) : MT);
      const float mb = stats[(size_t)(b * NMT + t) * 2 * D_ + tid];
      const float vb = stats[(size_t)(b * NMT + t) * 2 * D_ + D_ + tid];
      const float nn = n + nb;
      const float d = mb - mean;
      mean += d * (nb / nn);
      m2 += vb + d * d * (n * nb / nn);
      n = nn;
    }
    const float rstd = 1.0f / sqrtf(m2 / (float)L_ + 1e-5f);
    const float g = bf2f(((const unsigned short*)gg)[tid]);
    const float be = bf2f(((const unsigned short*)beg)[tid]);
    sc[tid] = rstd * g;
    sh[tid] = be - mean * rstd * g;
  }
  __syncthreads();
  const int c8 = (tid & 31) * 8;
  float s8[8], h8[8];
#pragma unroll
  for (int e = 0; e < 8; ++e) { s8[e] = sc[c8 + e]; h8[e] = sh[c8 + e]; }
#pragma unroll
  for (int i = 0; i < RPB / 8; ++i) {
    const int l = rb * RPB + i * 8 + (tid >> 5);
    if (l < L_) {
      const size_t off = ((size_t)(b * L_ + l)) * D_ + c8;
      const f32x4 v0 = *(const f32x4*)(y + off);
      const f32x4 v1 = *(const f32x4*)(y + off + 4);
      __hip_bfloat16 o[8];
#pragma unroll
      for (int e = 0; e < 8; ++e) {
        const float yv = e < 4 ? v0[e] : v1[e - 4];
        const float z = yv * s8[e] + h8[e];
        const float inner = 0.7978845608028654f * (z + 0.044715f * z * z * z);
        o[e] = __float2bfloat16(z / (1.0f + __expf(-2.0f * inner)));
      }
      *(uint4*)(out + off) = *(const uint4*)o;
    }
  }
}

extern "C" void kernel_launch(void* const* d_in, const int* in_sizes, int n_in,
                              void* d_out, int out_size, void* d_ws, size_t ws_size,
                              hipStream_t stream) {
  const __hip_bfloat16* x = (const __hip_bfloat16*)d_in[0];
  const __hip_bfloat16* w = (const __hip_bfloat16*)d_in[1];
  const __hip_bfloat16* bb = (const __hip_bfloat16*)d_in[2];
  const __hip_bfloat16* gamma = (const __hip_bfloat16*)d_in[3];
  const __hip_bfloat16* beta = (const __hip_bfloat16*)d_in[4];
  float* y = (float*)d_ws;
  float* stats = y + (size_t)B_ * L_ * D_;
  hipFuncSetAttribute((const void*)conv_gemm_kernel, hipFuncAttributeMaxDynamicSharedMemorySize, LDS_TOTAL);
  conv_gemm_kernel<<<B_ * NMT * 8, 256, LDS_TOTAL, stream>>>(x, w, bb, y, stats);
  norm_gelu_kernel<<<B_ * NB2, 256, 0, stream>>>(y, stats, gamma, beta, (__hip_bfloat16*)d_out);
}
```

```cpp
#include <hip/hip_runtime.h>
#include <hip/hip_bf16.h>

constexpr int B_ = 2, T_ = 4000, D_ = 256, KT = 31, L_ = 1985;
constexpr int MT = 128;
constexpr int NT = 32;
constexpr int NMT = 16;
constexpr int PLANE = 144 * 128;
constexpr int XBUF = 2 * PLANE;
constexpr int WSLOTS = 16;
constexpr int WRING = WSLOTS * 1024;
constexpr int LDS_W = 2 * XBUF;
constexpr int LDS_SCR = LDS_W + 4 * WRING;
constexpr int LDS_TOTAL = LDS_SCR + 4096;
constexpr int NSTEP = 4 * KT;

typedef __attribute__((ext_vector_type(8))) short bf16x8;
typedef __attribute__((ext_vector_type(4))) short s16x4;
typedef __attribute__((ext_vector_type(16))) float f32x16;
typedef __attribute__((ext_vector_type(4))) float f32x4;
typedef __attribute__((address_space(3))) s16x4* lds_s16x4;
typedef __attribute__((address_space(3))) unsigned* lds_u32;

#define WAIT_V(n) asm volatile("s_waitcnt vmcnt(" #n ")" ::: "memory")
#define WAIT_L0() asm volatile("s_waitcnt lgkmcnt(0)" ::: "memory")

__device__ __forceinline__ void glds16(const void* src, unsigned ldsaddr) {
  unsigned keep;
  asm volatile("s_mov_b32 %0, m0\n\ts_mov_b32 m0, %2\n\ts_nop 0\n\tglobal_load_lds_dwordx4 %1, off\n\ts_mov_b32 m0, %0"
               : "=&s"(keep) : "v"(src), "s"(ldsaddr) : "memory");
}
template <int I> struct IC { static constexpr int value = I; };
template <int I, int N, class F> __device__ __forceinline__ void static_for(F&& f) {
  if constexpr (I < N) { f(IC<I>{}); static_for<I + 1, N>(f); }
}
#define STAMP(i)
__device__ __forceinline__ float bf2f(unsigned short u) { return __uint_as_float(((unsigned)u) << 16); }

__global__ __launch_bounds__(256, 1)
void conv_gemm_kernel(const __hip_bfloat16* __restrict__ xg, const __hip_bfloat16* __restrict__ wg,
                      const __hip_bfloat16* __restrict__ bg, float* __restrict__ y, float* __restrict__ stats) {
  extern __shared__ __attribute__((aligned(16))) char smem[];
  const int tid = threadIdx.x;
  const int lane = tid & 63;
  const int w = __builtin_amdgcn_readfirstlane(tid >> 6);
  const int bid = blockIdx.x;
  const int ntile = bid & 7;
  const int mt = bid >> 3;
  const int b = mt >> 4;
  const int mtile = mt & 15;
  const int l0 = mtile * MT;
  const int n0 = ntile * NT;
  STAMP(0);

  const char* xb = (const char*)(xg + (size_t)b * T_ * D_);
  const char* wb = (const char*)wg;
  const unsigned lbase = (unsigned)(size_t)smem;

  auto load_x_piece = [&](int cc, int buf, int i) {
    {
      const int piece = w * 9 + i;
      const int p = piece >= 18 ? 1 : 0;
      const int u0 = (piece - p * 18) * 8;
      const int u = u0 + (lane >> 3);
      int t = 2 * l0 + 2 * u + p;
      t = t < T_ ? t : T_ - 1;
      const int j = (lane & 7) ^ ((u >> 1) & 7);
      const char* src = xb + (size_t)t * (D_ * 2) + cc * 128 + j * 16;
      glds16(src, __builtin_amdgcn_readfirstlane(lbase + buf * XBUF + p * PLANE + u0 * 128));
    }
  };
  auto load_x = [&](int cc, int buf) {
#pragma unroll
    for (int i = 0; i < 9; ++i) load_x_piece(cc, buf, i);
  };
  const unsigned wvoff = (unsigned)((w * 16 + (lane >> 2)) * (D_ * 2) + n0 * 2 + (lane & 3) * 16);
  const unsigned wring = __builtin_amdgcn_readfirstlane(lbase + LDS_W + w * WRING);
  auto load_w = [&](int cc, int tap) {
    const char* sb = wb + (size_t)(tap * D_ + cc * 64) * (D_ * 2);
    asm volatile("s_mov_b32 m0, %2\n\ts_nop 0\n\tglobal_load_lds_dwordx4 %0, %1"
                 :: "v"(wvoff), "s"(sb), "s"(wring + (tap & (WSLOTS - 1)) * 1024) : "memory");
  };

  f32x16 acc[4];
#pragma unroll
  for (int m = 0; m < 4; ++m)
#pragma unroll
    for (int i = 0; i < 16; ++i) acc[m][i] = 0.f;

  const int r = lane & 31, h = lane >> 5;
  const int jj = 2 * w + h;
  const int blk = (lane >> 4) & 1, q = (lane & 15) >> 2, pp = lane & 3;
  char* const bbase = smem + LDS_W + w * WRING + (8 * h + q) * 64 + blk * 32 + pp * 8;
  int aoff[16];
#pragma unroll
  for (int th = 0; th < 16; ++th) {
    const int u = r + th;
    aoff[th] = u * 128 + ((jj ^ ((u >> 1) & 7)) << 4);
  }

  load_x(0, 0);

#pragma unroll
  for (int s = 0; s < WSLOTS - 1; ++s) load_w(0, s);
  load_x(1, 1);
#pragma unroll
  for (int i = 0; i < 4; ++i) {
    int kr = mt * 248 + w * 62 + i * 16 + (lane >> 2);
    kr = kr < KT * D_ ? kr : KT * D_ - 1;
    glds16(wb + (size_t)kr * (D_ * 2) + n0 * 2 + (lane & 3) * 16,
           __builtin_amdgcn_readfirstlane(lbase + LDS_SCR + w * 1024));
  }
  WAIT_V(27);
  __builtin_amdgcn_s_barrier();
  STAMP(1);
  bf16x8 a[2][4]; s16x4 b0[2], b1[2];
#pragma unroll
  for (int m = 0; m < 4; ++m) a[0][m] = *(const bf16x8*)(smem + aoff[0] + m * 4096);
  b0[0] = __builtin_amdgcn_ds_read_tr16_b64_v4i16((lds_s16x4)bbase);
  b1[0] = __builtin_amdgcn_ds_read_tr16_b64_v4i16((lds_s16x4)(bbase + 256));

#define SB __builtin_amdgcn_sched_barrier(0)
#pragma unroll 1
  for (int cc = 0; cc < 4; ++cc) {
    const int ccn = (cc + 1) & 3;
    static_for<0, KT>([&](auto TAP) {
      constexpr int tap = decltype(TAP)::value;
      constexpr int cur = tap & 1;
      constexpr int ntap = (tap + 1) % KT;
      constexpr int nb = (tap + 1 == KT) ? 0 : (cur ^ 1);
      if constexpr (ntap == 0) {
        WAIT_V(14);
        WAIT_L0();
        __builtin_amdgcn_s_barrier();
#pragma unroll
        for (int th = 0; th < 16; ++th) aoff[th] += (cc & 1) ? -XBUF : XBUF;
      }
      const char* ap = smem + aoff[ntap >> 1] + (ntap & 1) * PLANE;
      char* bp = bbase + (ntap & (WSLOTS - 1)) * 1024;
      bf16x8 bf;
      bf[0] = b0[cur][0]; bf[1] = b0[cur][1]; bf[2] = b0[cur][2]; bf[3] = b0[cur][3];
      bf[4] = b1[cur][0]; bf[5] = b1[cur][1]; bf[6] = b1[cur][2]; bf[7] = b1[cur][3];
      bf16x8 c0 = a[cur][0], c1 = a[cur][1], c2 = a[cur][2], c3 = a[cur][3];
      SB;
      acc[0] = __builtin_amdgcn_mfma_f32_32x32x16_bf16(c0, bf, acc[0], 0, 0, 0);
      SB;
      WAIT_V(13);
      b0[nb] = __builtin_amdgcn_ds_read_tr16_b64_v4i16((lds_s16x4)bp);
      b1[nb] = __builtin_amdgcn_ds_read_tr16_b64_v4i16((lds_s16x4)(bp + 256));
      a[nb][0] = *(const bf16x8*)(ap);
      SB;
      acc[1] = __builtin_amdgcn_mfma_f32_32x32x16_bf16(c1, bf, acc[1], 0, 0, 0);
      SB;
      a[nb][1] = *(const bf16x8*)(ap + 4096);
      a[nb][2] = *(const bf16x8*)(ap + 8192);
      SB;
      acc[2] = __builtin_amdgcn_mfma_f32_32x32x16_bf16(c2, bf, acc[2], 0, 0, 0);
      SB;
      a[nb][3] = *(const bf16x8*)(ap + 12288);
      SB;
      acc[3] = __builtin_amdgcn_mfma_f32_32x32x16_bf16(c3, bf, acc[3], 0, 0, 0);
      SB;
      if constexpr (tap + WSLOTS - 1 < KT) load_w(cc, tap + WSLOTS - 1);
      else load_w(ccn, tap + WSLOTS - 1 - KT);
      if constexpr (tap >= 1 && tap <= 9) { if (cc >= 1 && cc < 3) load_x_piece(cc + 1, (cc + 1) & 1, tap - 1); }
      asm volatile("" :: "v"(c0), "v"(c1), "v"(c2), "v"(c3), "v"(bf));
      SB;
    });
  }

  STAMP(2);
#define LBAR() do { WAIT_L0(); __builtin_amdgcn_s_barrier(); } while (0)
  LBAR();
#pragma unroll
  for (int m = 0; m < 4; ++m) {
    if (m != w) {
#pragma unroll
      for (int g = 0; g < 4; ++g) {
        f32x4 t = {acc[m][4 * g], acc[m][4 * g + 1], acc[m][4 * g + 2], acc[m][4 * g + 3]};
        *(f32x4*)(smem + (((m * 4 + w) * 4 + g) * 64 + lane) * 16) = t;
      }
    }
  }
  f32x16 fin;
  if (w == 0) fin = acc[0]; else if (w == 1) fin = acc[1]; else if (w == 2) fin = acc[2]; else fin = acc[3];
  const float bias = bf2f(((const unsigned short*)bg)[n0 + r]);
  LBAR();
#pragma unroll
  for (int sw = 1; sw < 4; ++sw) {
    const int src = (w + sw) & 3;
#pragma unroll
    for (int g = 0; g < 4; ++g) {
      const f32x4 t = *(const f32x4*)(smem + (((w * 4 + src) * 4 + g) * 64 + lane) * 16);
      fin[4 * g] += t[0]; fin[4 * g + 1] += t[1]; fin[4 * g + 2] += t[2]; fin[4 * g + 3] += t[3];
    }
  }
  const int nvalid = (L_ - l0) < MT ? (L_ - l0) : MT;
  int nw = nvalid - 32 * w; nw = nw < 0 ? 0 : (nw > 32 ? 32 : nw);
  float s1 = 0.f;
  float* yp = y + ((size_t)(b * L_ + l0 + 32 * w)) * D_ + n0 + r;
#pragma unroll
  for (int i = 0; i < 16; ++i) {
    const int row = (i & 3) + 8 * (i >> 2) + 4 * h;
    fin[i] += bias;
    if (row < nw) { s1 += fin[i]; yp[(size_t)row * D_] = fin[i]; }
  }
  s1 += __shfl_xor(s1, 32);
  const float meanw = nw > 0 ? s1 / (float)nw : 0.f;
  float s2 = 0.f;
#pragma unroll
  for (int i = 0; i < 16; ++i) {
    const int row = (i & 3) + 8 * (i >> 2) + 4 * h;
    const float d = fin[i] - meanw;
    if (row < nw) s2 += d * d;
  }
  s2 += __shfl_xor(s2, 32);
  float* sred = (float*)(smem + 65536);
  if (lane < 32) { sred[w * 64 + r] = meanw; sred[w * 64 + 32 + r] = s2; }
  LBAR();
  if (tid < 32) {
    float n = 0.f, mean = 0.f, m2 = 0.f;
#pragma unroll
    for (int ww = 0; ww < 4; ++ww) {
      int nb_i = nvalid - 32 * ww; nb_i = nb_i < 0 ? 0 : (nb_i > 32 ? 32 : nb_i);
      if (nb_i > 0) {
        const float nb = (float)nb_i, nn = n + nb;
        const float d = sred[ww * 64 + tid] - mean;
        mean += d * (nb / nn);
        m2 += sred[ww * 64 + 32 + tid] + d * d * (n * nb / nn);
        n = nn;
      }
    }
    float* sp = stats + (size_t)mt * 2 * D_;
    sp[n0 + tid] = mean;
    sp[D_ + n0 + tid] = m2;
  }
  WAIT_V(0);
  STAMP(3);
}

constexpr int RPB = 8;
constexpr int NB2 = (L_ + RPB - 1) / RPB;

__global__ __launch_bounds__(256)
void norm_gelu_kernel(const float* __restrict__ y, const float* __restrict__ stats,
                      const __hip_bfloat16* __restrict__ gg, const __hip_bfloat16* __restrict__ beg,
                      __hip_bfloat16* __restrict__ out) {
  __shared__ float sc[D_], sh[D_];
  const int tid = threadIdx.x;
  const int b = blockIdx.x / NB2;
  const int rb = blockIdx.x - b * NB2;
  {
    float n = 0.f, mean = 0.f, m2 = 0.f;
#pragma unroll
    for (int t = 0; t < NMT; ++t) {
      const float nb = (float)((L_ - t * MT) < MT ? (L_ - t * MT) : MT);
      const float mb = stats[(size_t)(b * NMT + t) * 2 * D_ + tid];
      const float vb = stats[(size_t)(b * NMT + t) * 2 * D_ + D_ + tid];
      const float nn = n + nb;
      const float d = mb - mean;
      mean += d * (nb / nn);
      m2 += vb + d * d * (n * nb / nn);
      n = nn;
    }
    const float rstd = 1.0f / sqrtf(m2 / (float)L_ + 1e-5f);
    const float g = bf2f(((const unsigned short*)gg)[tid]);
    const float be = bf2f(((const unsigned short*)beg)[tid]);
    sc[tid] = rstd * g;
    sh[tid] = be - mean * rstd * g;
  }
  __syncthreads();
  const int c8 = (tid & 31) * 8;
  float s8[8], h8[8];
#pragma unroll
  for (int e = 0; e < 8; ++e) { s8[e] = sc[c8 + e]; h8[e] = sh[c8 + e]; }
#pragma unroll
  for (int i = 0; i < RPB / 8; ++i) {
    const int l = rb * RPB + i * 8 + (tid >> 5);
    if (l < L_) {
      const size_t off = ((size_t)(b * L_ + l)) * D_ + c8;
      const f32x4 v0 = *(const f32x4*)(y + off);
      const f32x4 v1 = *(const f32x4*)(y + off + 4);
      __hip_bfloat16 o[8];
#pragma unroll
      for (int e = 0; e < 8; ++e) {
        const float yv = e < 4 ? v0[e] : v1[e - 4];
        const float z = yv * s8[e] + h8[e];
        const float inner = 0.7978845608028654f * (z + 0.044715f * z * z * z);
        o[e] = __float2bfloat16(z / (1.0f + __expf(-2.0f * inner)));
      }
      *(uint4*)(out + off) = *(const uint4*)o;
    }
  }
}

extern "C" void kernel_launch(void* const* d_in, const int* in_sizes, int n_in,
                              void* d_out, int out_size, void* d_ws, size_t ws_size,
                              hipStream_t stream) {
  const __hip_bfloat16* x = (const __hip_bfloat16*)d_in[0];
  const __hip_bfloat16* w = (const __hip_bfloat16*)d_in[1];
  const __hip_bfloat16* bb = (const __hip_bfloat16*)d_in[2];
  const __hip_bfloat16* gamma = (const __hip_bfloat16*)d_in[3];
  const __hip_bfloat16* beta = (const __hip_bfloat16*)d_in[4];
  float* y = (float*)d_ws;
  float* stats = y + (size_t)B_ * L_ * D_;
  hipFuncSetAttribute((const void*)conv_gemm_kernel, hipFuncAttributeMaxDynamicSharedMemorySize, LDS_TOTAL);
  conv_gemm_kernel<<<B_ * NMT * 8, 256, LDS_TOTAL, stream>>>(x, w, bb, y, stats);
  norm_gelu_kernel<<<B_ * NB2, 256, 0, stream>>>(y, stats, gamma, beta, (__hip_bfloat16*)d_out);
}
```
